```python
import torch

def _softmax(x, axis=-1):
    return torch.softmax(x, dim=axis)
def _gelu(x):
    return torch.nn.functional.gelu(x, approximate="tanh")
def _silu(x):
    return torch.nn.functional.silu(x)
def _sigmoid(x):
    return torch.sigmoid(x)
def _layernorm(x, axis=-1, eps=1e-5):
    m = x.mean(dim=axis, keepdim=True)
    v = x.var(dim=axis, keepdim=True, unbiased=False)
    return (x - m) / torch.sqrt(v + eps)
def _scatter_add(tgt, idx, src, axis=0):
    return tgt.clone().index_add_(axis, idx, src)
def _segment_sum(src, seg, n):
    out = torch.zeros((n,) + src.shape[1:], dtype=src.dtype)
    return out.index_add_(0, seg, src)
def _pad(x, pw):
    flat = tuple(v for pair in reversed(pw) for v in pair)
    return torch.nn.functional.pad(x, flat)

kernel_name = 'sequential_preLN__mha_block_cached__mixer_token_mlp__decode'

def setup_inputs():

    _w = (lambda a: (lambda t: t.to(torch.bfloat16) if t.is_floating_point() else t)(torch.as_tensor(a)))
    def _A_setup():
        B, Sq, D = 4, 1, 640
        H, Sk, Dh = 32, 4096, 192
        return {
            "x": _w((lambda *s: torch.randn(*s, dtype=torch.bfloat16) * 0.05)(B, Sq, D)),
            "k_cache": _w((lambda *s: torch.randn(*s, dtype=torch.bfloat16) * 1.6)(B, H, Sk, Dh)),
            "v_cache": _w((lambda *s: torch.randn(*s, dtype=torch.bfloat16) * 0.8)(B, H, Sk, Dh)),
            "wq": _w((lambda *s: torch.randn(*s, dtype=torch.bfloat16) * 1.6)(D, H * Dh) * (D ** -0.5)),
            "wo": _w((lambda *s: torch.randn(*s, dtype=torch.bfloat16) * 0.8)(H * Dh, D) * ((H * Dh) ** -0.5)),
        }
    _A_in = _A_setup()

    def _B_setup():
        B, S, D, F = 4, 1, 640, 1536
        return {
            "x": _w((lambda *s: torch.randn(*s, dtype=torch.bfloat16) * 0.05)(B, S, D)),
            "w1": _w((lambda *s: torch.randn(*s, dtype=torch.bfloat16) * 3.2)(S, F) * (S ** -0.5)),
            "b1": _w((lambda *s: torch.randn(*s, dtype=torch.bfloat16) * 3.2)(F) * 0.02),
            "w2": _w((lambda *s: torch.randn(*s, dtype=torch.bfloat16) * 0.4)(F, S) * (F ** -0.5)),
            "b2": _w((lambda *s: torch.randn(*s, dtype=torch.bfloat16) * 0.4)(S) * 0.02),
        }
    _B_in = _B_setup()

    return {
        "A_x": _A_in["x"],
        "A_k_cache": _A_in["k_cache"],
        "A_v_cache": _A_in["v_cache"],
        "A_wq": _A_in["wq"],
        "A_wo": _A_in["wo"],
        "B_x": _B_in["x"],
        "B_w1": _B_in["w1"],
        "B_b1": _B_in["b1"],
        "B_w2": _B_in["w2"],
        "B_b2": _B_in["b2"],
    }

def reference(A_x, A_k_cache, A_v_cache, A_wq, A_wo, B_x, B_w1, B_b1, B_w2, B_b2):
    def _A_ref(x, k_cache, v_cache, wq, wo):
        B, Sq, D = x.shape
        H, Dh = 32, 192
        q = (lambda x, *d: x.permute(*d))((x @ wq).reshape(B, Sq, H, Dh), 0, 2, 1, 3)
        scores = torch.einsum("bhqd,bhkd->bhqk", q, k_cache) * (Dh ** -0.5)
        p = _softmax(scores, axis=-1)
        o = torch.einsum("bhqk,bhkd->bhqd", p, v_cache)
        return (lambda x, *d: x.permute(*d))(o, 0, 2, 1, 3).reshape(B, Sq, H * Dh) @ wo

    def _B_ref(x, w1, b1, w2, b2):
        B, S, D = x.shape
        h = _layernorm(x, axis=-1)
        u = torch.einsum("bsd,sf->bdf", h, w1) + b1
        u = _gelu(u)
        y = torch.einsum("bdf,fs->bds", u, w2) + b2
        return (lambda x, *d: x.permute(*d))(y, 0, 2, 1)

    x = A_x
    h = x + _A_ref(_layernorm(x, axis=-1), A_k_cache, A_v_cache, A_wq, A_wo)
    h2 = h + _B_ref(_layernorm(h, axis=-1), B_w1, B_b1, B_w2, B_b2)
    return h2
```

```cpp
#include <hip/hip_runtime.h>
#include <hip/hip_bf16.h>

#define MIMIC 0
typedef unsigned v4u __attribute__((ext_vector_type(4)));
typedef __bf16 bf16x2 __attribute__((ext_vector_type(2)));
typedef unsigned short u16;
constexpr int NB = 4, D = 640, H = 32, SK = 4096, DH = 192, HD = H * DH, F = 1536;
constexpr unsigned KVB = (unsigned)NB * H * SK * DH * 2u, SLAB = SK * DH * 2;

__device__ __forceinline__ float bf2f(u16 v) { return __builtin_bit_cast(float, (unsigned)v << 16); }
__device__ __forceinline__ float rbf(float x) { return (float)(__bf16)x; }
__device__ __forceinline__ float mrbf(float x) { return MIMIC ? rbf(x) : x; }
__device__ __forceinline__ u16 f2bf(float x) { return __builtin_bit_cast(u16, (__bf16)x); }
__device__ __forceinline__ float dot2(unsigned a, unsigned b, float c) {
  return __builtin_amdgcn_fdot2_f32_bf16(__builtin_bit_cast(bf16x2, a), __builtin_bit_cast(bf16x2, b), c, false); }
__device__ __forceinline__ float wsum(float v) { for (int o = 32; o; o >>= 1) v += __shfl_xor(v, o); return v; }
__device__ __forceinline__ float wmax(float v) { for (int o = 32; o; o >>= 1) v = fmaxf(v, __shfl_xor(v, o)); return v; }

__device__ __forceinline__ void ln10(float (&v)[10]) {
  float s = 0; for (int i = 0; i < 10; ++i) s += v[i];
  const float mean = wsum(s) * (1.f / D);
  float q = 0; for (int i = 0; i < 10; ++i) { const float d = v[i] - mean; q += d * d; }
  const float var = mrbf(wsum(q) * (1.f / D)), m = mrbf(mean);
  const float sd = mrbf(sqrtf(mrbf(var + 1e-5f)));
  for (int i = 0; i < 10; ++i) v[i] = mrbf(mrbf(v[i] - m) / sd);
}

__global__ void __launch_bounds__(256) k_q(const u16* x, const u16* wq, u16* q) {
  __shared__ float xn[NB][D];
  __shared__ float red[4][NB][64];
  const int lane = threadIdx.x & 63, w = threadIdx.x >> 6;
  float v[10]; for (int i = 0; i < 10; ++i) v[i] = bf2f(x[w * D + i * 64 + lane]);
  ln10(v);
  for (int i = 0; i < 10; ++i) xn[w][i * 64 + lane] = rbf(v[i]);
  __syncthreads();
  const int col = blockIdx.x * 64 + lane;
  float a[NB] = {0, 0, 0, 0};
  for (int d = w * 160; d < w * 160 + 160; ++d) { const float wv = bf2f(wq[(size_t)d * HD + col]); for (int b = 0; b < NB; ++b) a[b] += xn[b][d] * wv; }
  for (int b = 0; b < NB; ++b) red[w][b][lane] = a[b];
  __syncthreads();
  q[w * HD + col] = f2bf(red[0][w][lane] + red[1][w][lane] + red[2][w][lane] + red[3][w][lane]);
}

template<int CTRL> __device__ __forceinline__ float dpp(float x) {
  return __builtin_bit_cast(float, __builtin_amdgcn_mov_dpp(__builtin_bit_cast(int, x), CTRL, 0xf, 0xf, true)); }

__global__ void __launch_bounds__(256) k_attn(const u16* q, const char* K, const char* V, float* part) {
  const int wg = blockIdx.x, lane = threadIdx.x & 63, h = wg & 31, m = wg >> 5;
  const int wave = __builtin_amdgcn_readfirstlane(threadIdx.x >> 6);
  const unsigned loff = (lane >> 3) * 384 + (lane & 7) * 16;
  const unsigned wbase = h * SLAB + (m * 4 + wave) * 3072;
  const __amdgpu_buffer_rsrc_t rk = __builtin_amdgcn_make_buffer_rsrc((void*)K, 0, (int)KVB, 0x00020000);
  const __amdgpu_buffer_rsrc_t rv = __builtin_amdgcn_make_buffer_rsrc((void*)V, 0, (int)KVB, 0x00020000);
  constexpr int R = 4, W = 2;
  v4u a[R][3];
  auto issue = [&](v4u (&r)[3], const __amdgpu_buffer_rsrc_t& rs, unsigned so) {
#pragma unroll
    for (int j = 0; j < 3; ++j) r[j] = __builtin_amdgcn_raw_buffer_load_b128(rs, loff + j * 128, so, 2);
    __builtin_amdgcn_sched_barrier(0); };
#pragma unroll
  for (int i = 0; i < W; ++i) issue(a[i], rk, wbase + i * 98304);
  constexpr float C = 0.07216878364870322f, L2E = 1.4426950408889634f;
#pragma unroll 1
  for (int b = 0; b < NB; ++b) {
    const unsigned sb = wbase + b * (32 * SLAB);
    v4u qv[3];
#pragma unroll
    for (int j = 0; j < 3; ++j) qv[j] = *(const v4u*)(q + b * HD + h * DH + ((lane & 7) + 8 * j) * 8);
    float s[16];
#pragma unroll
    for (int g = 0; g < 16; ++g) {
      asm volatile("s_waitcnt vmcnt(%0)" :: "i"(3 * (W - 1)) : "memory");
      __builtin_amdgcn_sched_barrier(0);
      if (g + W < 16) issue(a[(g + W) % R], rk, sb + (g + W) * 98304); else issue(a[(g + W) % R], rv, sb + (g + W - 16) * 98304);
      float d0 = 0, d1 = 0, d2 = 0;
#pragma unroll
      for (int k = 0; k < 4; ++k) { d0 = dot2(a[g % R][0][k], qv[0][k], d0); d1 = dot2(a[g % R][1][k], qv[1][k], d1); d2 = dot2(a[g % R][2][k], qv[2][k], d2); }
      float d = d0 + d1 + d2;
      d += dpp<0xB1>(d); d += dpp<0x4E>(d); d += dpp<0x141>(d);
      s[g] = mrbf(mrbf(d) * C);
      asm volatile("" : "+v"(s[g]));
      __builtin_amdgcn_sched_barrier(0);
    }
    float mx = s[0];
#pragma unroll
    for (int g = 1; g < 16; ++g) mx = fmaxf(mx, s[g]);
    mx = wmax(mx);
    float l = 0;
#pragma unroll
    for (int g = 0; g < 16; ++g) { s[g] = __builtin_amdgcn_exp2f((s[g] - mx) * L2E); l += s[g]; }
    l = wsum(l) * 0.125f;
    float acc[24];
#pragma unroll
    for (int k = 0; k < 24; ++k) acc[k] = 0;
    const unsigned nb = b + 1 < NB ? sb + 32 * SLAB : 0;
    const __amdgpu_buffer_rsrc_t rn = __builtin_amdgcn_make_buffer_rsrc((void*)K, 0, b + 1 < NB ? (int)KVB : 0, 0x00020000);
#pragma unroll
    for (int g = 0; g < 16; ++g) {
      asm volatile("s_waitcnt vmcnt(%0)" :: "i"(3 * (W - 1)) : "memory");
      __builtin_amdgcn_sched_barrier(0);
      if (g + W < 16) issue(a[(g + W) % R], rv, sb + (g + W) * 98304); else issue(a[(g + W) % R], rn, nb + (g + W - 16) * 98304);
#pragma unroll
      for (int j = 0; j < 3; ++j)
#pragma unroll
        for (int k = 0; k < 4; ++k) { const unsigned u = a[g % R][j][k];
          acc[j * 8 + 2 * k] = fmaf(s[g], __builtin_bit_cast(float, u << 16), acc[j * 8 + 2 * k]);
          acc[j * 8 + 2 * k + 1] = fmaf(s[g], __builtin_bit_cast(float, u & 0xffff0000u), acc[j * 8 + 2 * k + 1]); }
#pragma unroll
      for (int k = 0; k < 24; ++k) asm volatile("" : "+v"(acc[k]));
      __builtin_amdgcn_sched_barrier(0);
    }
#pragma unroll
    for (int k = 0; k < 24; ++k) { float t = acc[k]; t += __shfl_xor(t, 8); t += __shfl_xor(t, 16); t += __shfl_xor(t, 32); acc[k] = t; }
    float* p = part + ((((size_t)b * H + h) * 8 + m) * 4 + wave) * 196;
    if (lane < 8) {
#pragma unroll
      for (int j = 0; j < 3; ++j)
#pragma unroll
        for (int e = 0; e < 8; ++e) p[(lane + 8 * j) * 8 + e] = acc[j * 8 + e];
      if (lane == 0) { p[192] = mx; p[193] = l; } }
  }
}

__global__ void __launch_bounds__(256) k_wo(const float* part, const u16* wo, float* y) {
  __shared__ float o[NB][DH];
  __shared__ float red[3][NB][80];
  const int h = blockIdx.x & 31, m = blockIdx.x >> 5, t = threadIdx.x;
  for (int i = t; i < NB * DH; i += 256) { const int b = i / DH, d = i % DH;
    const float* p = part + ((size_t)b * H + h) * 32 * 196;
    float M = -INFINITY; for (int s = 0; s < 32; ++s) M = fmaxf(M, p[s * 196 + 192]);
    float L = 0, a = 0; for (int s = 0; s < 32; ++s) { const float w = __expf(p[s * 196 + 192] - M); L += w * p[s * 196 + 193]; a += w * p[s * 196 + d]; }
    o[b][d] = rbf(a / L); }
  __syncthreads();
  if (t < 240) { const int c = t % 80, ks = t / 80; float a[NB] = {0, 0, 0, 0};
    for (int k = ks * 64; k < ks * 64 + 64; ++k) { const float w = bf2f(wo[(size_t)(h * DH + k) * D + m * 80 + c]); for (int b = 0; b < NB; ++b) a[b] += o[b][k] * w; }
    for (int b = 0; b < NB; ++b) red[ks][b][c] = a[b]; }
  __syncthreads();
  for (int i = t; i < NB * 80; i += 256) { const int b = i / 80, c = i % 80; y[((size_t)h * NB + b) * D + m * 80 + c] = red[0][b][c] + red[1][b][c] + red[2][b][c]; }
}

__global__ void __launch_bounds__(256) k_mlp(const float* y, const u16* x, const u16* w1, const u16* b1, const u16* w2, const u16* b2, u16* out) {
  __shared__ float hs[D], hn[D], red[4][10];
  const int b = blockIdx.x >> 6, c0 = (blockIdx.x & 63) * 10, t = threadIdx.x, lane = t & 63, w = t >> 6;
  for (int d = t; d < D; d += 256) { float a = 0; for (int h = 0; h < H; ++h) a += y[((size_t)h * NB + b) * D + d]; hs[d] = mrbf(bf2f(x[b * D + d]) + mrbf(a)); }
  __syncthreads();
  if (w == 0) { float v[10]; for (int i = 0; i < 10; ++i) v[i] = hs[i * 64 + lane]; ln10(v); ln10(v); for (int i = 0; i < 10; ++i) hn[i * 64 + lane] = v[i]; }
  __syncthreads();
  float a[10]; for (int c = 0; c < 10; ++c) a[c] = 0;
  for (int f = t; f < F; f += 256) { const float W1 = bf2f(w1[f]), B1 = bf2f(b1[f]), W2 = bf2f(w2[f]);
    for (int c = 0; c < 10; ++c) { const float u = mrbf(mrbf(hn[c0 + c] * W1) + B1);
      const float z = 1.5957691216057308f * (u + 0.044715f * u * u * u);
      const float g = mrbf(u / (1.f + __expf(-z))); a[c] += g * W2; } }
  for (int c = 0; c < 10; ++c) { const float s = wsum(a[c]); if (lane == 0) red[w][c] = s; }
  __syncthreads();
  if (t < 10) { const float yv = mrbf(mrbf(red[0][t] + red[1][t] + red[2][t] + red[3][t]) + bf2f(b2[0])); out[b * D + c0 + t] = f2bf(hs[c0 + t] + yv); }
}

extern "C" void kernel_launch(void* const* d_in, const int* in_sizes, int n_in, void* d_out, int out_size, void* d_ws, size_t ws_size, hipStream_t stream) {
  const u16* x = (const u16*)d_in[0];
  char* ws = (char*)d_ws;
  u16* q = (u16*)ws; float* part = (float*)(ws + 65536); float* y = (float*)(ws + 65536 + 4194304);
  k_q<<<HD / 64, 256, 0, stream>>>(x, (const u16*)d_in[3], q);
  k_attn<<<256, 256, 0, stream>>>(q, (const char*)d_in[1], (const char*)d_in[2], part);
  k_wo<<<256, 256, 0, stream>>>(part, (const u16*)d_in[4], y);
  k_mlp<<<256, 256, 0, stream>>>(y, x, (const u16*)d_in[6], (const u16*)d_in[7], (const u16*)d_in[8], (const u16*)d_in[9], (u16*)d_out);
}
```

```cpp
#include <hip/hip_runtime.h>
#include <hip/hip_bf16.h>

#define LAS __attribute__((address_space(3)))
typedef unsigned v4u __attribute__((ext_vector_type(4)));
typedef unsigned v2u __attribute__((ext_vector_type(2)));
typedef float v4f __attribute__((ext_vector_type(4)));
typedef float v2f __attribute__((ext_vector_type(2)));
typedef short v4s __attribute__((ext_vector_type(4)));
typedef __bf16 v8b __attribute__((ext_vector_type(8)));
typedef __bf16 v2b __attribute__((ext_vector_type(2)));
typedef __amdgpu_buffer_rsrc_t rsrc_t;

namespace {
constexpr unsigned SLAB = 4096u * 384u, KVB = 128u * SLAB, ROUND = 32u * 3072u;
constexpr unsigned WS_QP = 4096, WS_OP = WS_QP + 256 * 6144, WS_YP = WS_OP + 4 * 256 * 1024, WS_HP = WS_YP + 128 * 2560, WS_END = WS_HP + 4 * 2560;
constexpr int L_Q = 0, L_FLAG = 1536, L_RED = 1600, L_OB = 1664, L_PART = 2048, L_MLP = L_PART + 16 * 784, L_STG = L_MLP + 3 * 6144, L_TAIL = L_STG + 4 * 7680, L_END = L_TAIL + 4 * 5120;
constexpr int MASKED = 0x40000000, NT = 2, SC1 = 16;
constexpr int SPIN = 1 << 15;
constexpr int W = 2, NPRE = 8;
constexpr int NRED = 40, NMLP = 214;
constexpr float LOG2E = 1.4426950408889634f;
#define STAMP(i)

__device__ __forceinline__ rsrc_t mk(const void* p, unsigned n) { return __builtin_amdgcn_make_buffer_rsrc((void*)p, 0, (int)n, 0x00020000); }
__device__ __forceinline__ float asf(unsigned u) { return __builtin_bit_cast(float, u); }
__device__ __forceinline__ unsigned asu(float f) { return __builtin_bit_cast(unsigned, f); }
__device__ __forceinline__ float lo16(unsigned u) { return asf(u << 16); }
__device__ __forceinline__ float hi16(unsigned u) { return asf(u & 0xffff0000u); }
__device__ __forceinline__ unsigned pk(float a, float b) { return __builtin_bit_cast(unsigned, v2b{(__bf16)a, (__bf16)b}); }
__device__ __forceinline__ float rbf(float x) { return (float)(__bf16)x; }
__device__ __forceinline__ float dot2(unsigned a, unsigned b, float c) {
  return __builtin_amdgcn_fdot2_f32_bf16(__builtin_bit_cast(v2b, a), __builtin_bit_cast(v2b, b), c, false); }
template <int C> __device__ __forceinline__ float dpp(float x) { return asf(__builtin_amdgcn_mov_dpp(asu(x), C, 0xf, 0xf, true)); }
__device__ __forceinline__ float sw16(float a, float b) { auto r = __builtin_amdgcn_permlane16_swap(asu(a), asu(b), false, false); return asf(r[0]) + asf(r[1]); }
__device__ __forceinline__ float sw32(float a, float b) { auto r = __builtin_amdgcn_permlane32_swap(asu(a), asu(b), false, false); return asf(r[0]) + asf(r[1]); }
__device__ __forceinline__ float row_sum(float x) { x += dpp<0x128>(x); x += dpp<0x124>(x); x += dpp<0x122>(x); x += dpp<0x121>(x); return x; }
__device__ __forceinline__ float wave_sum(float x) { x = row_sum(x); x = sw16(x, x); return sw32(x, x); }
__device__ __forceinline__ float exp2_(float x) { return __builtin_amdgcn_exp2f(x); }
__device__ __forceinline__ float recip(float x) { const float r = __builtin_amdgcn_rcpf(x); return fmaf(fmaf(-x, r, 1.f), r, r); }
__device__ __forceinline__ float rdl(float x, int l) { return asf(__builtin_amdgcn_readlane(asu(x), l)); }
__device__ __forceinline__ void ln_consts(float mean, float var, float& m, float& rinv) {
  m = rbf(mean); rinv = recip(rbf(__builtin_sqrtf(rbf(rbf(var) + 1e-5f)))); }

struct Tag { unsigned lo, hi, t0, t1, t2, t3; };
__device__ __forceinline__ Tag make_tag(v2u e) {
  unsigned long long t = (((unsigned long long)__builtin_amdgcn_readfirstlane(e.y) << 32) | __builtin_amdgcn_readfirstlane(e.x)) + 1ull;
  return Tag{(unsigned)t, (unsigned)(t >> 32), (unsigned)t & 1023u, (unsigned)(t >> 10) & 1023u, (unsigned)(t >> 20) & 1023u, (unsigned)(t >> 30) & 1023u}; }
__device__ __forceinline__ v4u pack4(v4f v, const Tag& T) {
  return v4u{((asu(v.x) + 512u) & ~1023u) | T.t0, ((asu(v.y) + 512u) & ~1023u) | T.t1, ((asu(v.z) + 512u) & ~1023u) | T.t2, ((asu(v.w) + 512u) & ~1023u) | T.t3}; }
__device__ __forceinline__ unsigned bad4(v4u g, const Tag& T) { return ((g.x ^ T.t0) | (g.y ^ T.t1) | (g.z ^ T.t2) | (g.w ^ T.t3)) & 1023u; }
__device__ __forceinline__ v4f val4(v4u g) { return v4f{asf(g.x & ~1023u), asf(g.y & ~1023u), asf(g.z & ~1023u), asf(g.w & ~1023u)}; }

#define FENCE() asm volatile("" ::: "memory")
#define SB() __builtin_amdgcn_sched_barrier(0)
#define LDS_WAIT() asm volatile("s_waitcnt lgkmcnt(0)" ::: "memory")
#define BARRIER() asm volatile("s_waitcnt lgkmcnt(0)\n\ts_barrier" ::: "memory")
#define STREAM_WAIT() do { asm volatile("s_waitcnt vmcnt(%0)" ::"n"(3 * (W - 1)) : "memory"); SB(); } while (0)

__device__ __forceinline__ void lds_wait_ge(volatile LAS unsigned* p, unsigned want) {
  for (int i = 0; i < (SPIN << 4); ++i) { if (__builtin_amdgcn_readfirstlane(*p) >= want) break; __builtin_amdgcn_s_sleep(1); }
  FENCE(); }
}

struct Args { const void *x, *k, *v, *wq, *wo, *w1, *b1, *w2, *b2; void *out, *ws; };

__global__ void __launch_bounds__(512, 2) fused_decode(Args a) {
  __shared__ __attribute__((aligned(16))) unsigned char lds_raw[L_END];
  LAS unsigned char* L = (LAS unsigned char*)lds_raw;
  volatile LAS unsigned* flag = (volatile LAS unsigned*)(L + L_FLAG);
  const int wg = blockIdx.x, lane = threadIdx.x & 63, h = wg & 31, m = wg >> 5;
  const int wave = __builtin_amdgcn_readfirstlane(threadIdx.x >> 6);
  const rsrc_t rws = mk(a.ws, WS_END), rx = mk(a.x, 5120);
  STAMP(0);
  const v2u e0 = __builtin_amdgcn_raw_buffer_load_b64(rws, 0, 0, SC1);

  if (wave < 4) {
    const int w = wave;
    const unsigned loff = (lane >> 3) * 384 + (lane & 7) * 16;
    const unsigned wbase = h * SLAB + (m * 4 + w) * 3072;
    const rsrc_t rk = mk(a.k, KVB), rv = mk(a.v, KVB);
    v4u ring[4][3], pre[NPRE][3];
    auto issue = [&](v4u(&r)[3], const rsrc_t& rs, unsigned so) {
#pragma unroll
      for (int j = 0; j < 3; ++j) r[j] = __builtin_amdgcn_raw_buffer_load_b128(rs, loff + j * 128, so, NT);
      SB(); };
#pragma unroll
    for (int g = 0; g < NPRE + W; ++g) {
      if (g >= W) STREAM_WAIT();
      if (g < NPRE) issue(pre[g], rk, wbase + g * ROUND); else issue(ring[g % 4], rk, wbase + g * ROUND); }
    STAMP(1);
    BARRIER();
    STAMP(2);
    v4u q[3];
    float s[16];
    auto loadq = [&](int b) {
#pragma unroll
      for (int j = 0; j < 3; ++j) q[j] = *(const LAS v4u*)(L + L_Q + b * 384 + (lane & 7) * 16 + j * 128); };
    auto score = [&](const v4u(&r)[3]) -> float {
      float d0 = 0.f, d1 = 0.f, d2 = 0.f;
#pragma unroll
      for (int k = 0; k < 4; ++k) { d0 = dot2(r[0][k], q[0][k], d0); d1 = dot2(r[1][k], q[1][k], d1); d2 = dot2(r[2][k], q[2][k], d2); }
      float d = (d0 + d1) + d2;
      d += dpp<0xB1>(d); d += dpp<0x4E>(d); d += dpp<0x141>(d);
      d = rbf(rbf(d) * 0.07216878364870322f);
      asm volatile("" : "+v"(d));
      return d; };
    loadq(0);
#pragma unroll
    for (int g = NPRE; g < 16; ++g) {
      STREAM_WAIT();
      if (g + W < 16) issue(ring[(g + W) % 4], rk, wbase + (g + W) * ROUND); else issue(ring[(g + W) % 4], rv, wbase + (g + W - 16) * ROUND);
      s[g] = score(ring[g % 4]);
      s[g - NPRE] = score(pre[g - NPRE]);
      SB(); }
    const int b5 = lane >> 5, b4 = (lane >> 4) & 1, b3 = (lane >> 3) & 1, a0 = 12 * b5 + 6 * b4 + 3 * b3;
#pragma unroll 1
    for (int b = 0;; ++b) {
      const unsigned sb = wbase + b * (32 * SLAB);
      const rsrc_t rn = mk(a.k, b < 3 ? KVB : 0u);
      const unsigned nb = b < 3 ? sb + 32 * SLAB : 0u;
      float mx = 0.f, l = 0.f, acc[24];
#pragma unroll
      for (int g = 0; g < 16; ++g) {
        STREAM_WAIT();
        if (g + W < 16) issue(ring[(g + W) % 4], rv, sb + (g + W) * ROUND); else issue(ring[(g + W) % 4], rn, nb + (g + W - 16) * ROUND);
        if (g == 0) {
          mx = s[0];
#pragma unroll
          for (int i = 1; i < 16; ++i) mx = fmaxf(mx, s[i]);
          mx = fmaxf(mx, dpp<0x128>(mx));
          { auto r = __builtin_amdgcn_permlane16_swap(asu(mx), asu(mx), false, false); mx = fmaxf(asf(r[0]), asf(r[1])); }
          { auto r = __builtin_amdgcn_permlane32_swap(asu(mx), asu(mx), false, false); mx = fmaxf(asf(r[0]), asf(r[1])); }
#pragma unroll
          for (int i = 0; i < 16; ++i) { s[i] = exp2_((s[i] - mx) * LOG2E); l += s[i]; }
          l += dpp<0x128>(l); l = sw16(l, l); l = sw32(l, l);
#pragma unroll
          for (int k = 0; k < 24; ++k) acc[k] = 0.f; }
#pragma unroll
        for (int j = 0; j < 3; ++j)
#pragma unroll
          for (int k = 0; k < 4; ++k) { const unsigned u = ring[g % 4][j][k];
            acc[j * 8 + 2 * k] = fmaf(s[g], lo16(u), acc[j * 8 + 2 * k]);
            acc[j * 8 + 2 * k + 1] = fmaf(s[g], hi16(u), acc[j * 8 + 2 * k + 1]); }
#pragma unroll
        for (int k = 0; k < 24; ++k) asm volatile("" : "+v"(acc[k]));
        SB(); }
#pragma unroll
      for (int i = 0; i < 12; ++i) acc[i] = sw32(acc[i], acc[i + 12]);
#pragma unroll
      for (int i = 0; i < 6; ++i) acc[i] = sw16(acc[i], acc[i + 6]);
      LAS float* part = (LAS float*)(L + L_PART + (b * 4 + w) * 784);
#pragma unroll
      for (int t = 0; t < 3; ++t) { const float u0 = acc[t] + dpp<0x128>(acc[t]), u1 = acc[t + 3] + dpp<0x128>(acc[t + 3]);
        const int ai = a0 + t; part[64 * (ai >> 3) + 8 * (lane & 7) + (ai & 7)] = b3 ? u1 : u0; }
      if (lane == 0) { part[192] = mx; part[193] = l; }
      LDS_WAIT();
      if (lane == 0) __hip_atomic_fetch_add((LAS unsigned*)(L + L_FLAG) + b, 1u, __ATOMIC_RELAXED, __HIP_MEMORY_SCOPE_WORKGROUP);
      if (b == 3) break;
      loadq(b + 1);
#pragma unroll
      for (int g = 0; g < 16; ++g) {
        STREAM_WAIT();
        if (g + W < 16) issue(ring[(g + W) % 4], rk, nb + (g + W) * ROUND); else issue(ring[(g + W) % 4], rv, nb + (g + W - 16) * ROUND);
        s[g] = score(ring[g % 4]);
        SB(); }
    }
    if (wg < NMLP) {
      const Tag T = make_tag(e0);
      const float b2 = lo16(*(const unsigned short*)a.b2);
      LAS float* hb = (LAS float*)(L + L_TAIL + w * 5120);
      const int c0 = 3 * wg;
#pragma unroll 1
      for (int pass = (w == 3); pass < 2; ++pass) {
        const int b = pass ? 3 : w;
        v4u g[3];
        for (int i = 0; i < SPIN; ++i) {
          FENCE();
          g[0] = __builtin_amdgcn_raw_buffer_load_b128(rws, lane * 16, WS_HP + b * 2560, SC1);
          g[1] = __builtin_amdgcn_raw_buffer_load_b128(rws, lane * 16 + 1024, WS_HP + b * 2560, SC1);
          g[2] = __builtin_amdgcn_raw_buffer_load_b128(rws, lane < 32 ? lane * 16 + 2048 : MASKED, WS_HP + b * 2560, SC1);
          if (__all((bad4(g[0], T) | bad4(g[1], T) | (lane < 32 ? bad4(g[2], T) : 0u)) == 0u)) break;
          __builtin_amdgcn_s_sleep(1); }
        v4f v[3] = {val4(g[0]), val4(g[1]), val4(g[2])};
#pragma unroll
        for (int i = 0; i < 3; ++i) if (i < 2 || lane < 32) *(LAS v4f*)(hb + i * 256 + lane * 4) = v[i];
        const float live = lane < 32 ? 1.f : 0.f;
#pragma unroll
        for (int rep = 0; rep < 2; ++rep) {
          const v4f s4 = (v[0] + v[1]) + v[2];
          const float mean = wave_sum((s4.x + s4.y) + (s4.z + s4.w)) * (1.f / 640.f);
          const v4f d0 = v[0] - mean, d1 = v[1] - mean, d2 = (v[2] - mean) * live;
          const v4f q4 = d0 * d0 + d1 * d1 + d2 * d2;
          float mm, rinv; ln_consts(mean, wave_sum((q4.x + q4.y) + (q4.z + q4.w)) * (1.f / 640.f), mm, rinv);
#pragma unroll
          for (int i = 0; i < 3; ++i)
#pragma unroll
            for (int k = 0; k < 4; ++k) v[i][k] = rbf(rbf(v[i][k] - mm) * rinv);
          v[2] *= live; }
#pragma unroll
        for (int i = 0; i < 3; ++i) if (i < 2 || lane < 32) *(LAS v4f*)(hb + 640 + i * 256 + lane * 4) = v[i];
        float hn[3];
#pragma unroll
        for (int c = 0; c < 3; ++c) hn[c] = hb[640 + min(c0 + c, 639)];
        const float hres = hb[min(c0 + (lane & 3), 639)];
        v2f ac[3] = {{0.f, 0.f}, {0.f, 0.f}, {0.f, 0.f}};
        const int i0 = pass ? 3 * w : 0, i1 = pass ? 3 * w + 3 : 12;
#pragma unroll 1
        for (int i = i0; i < i1; ++i) {
          const LAS v2f* mw = (const LAS v2f*)(L + L_MLP) + i * 64 + lane;
          const v2f w1 = mw[0], b1 = mw[768], w2 = mw[1536];
#pragma unroll
          for (int c = 0; c < 3; ++c) {
            const v2f u = hn[c] * w1 + b1;
            const v2f z = u * (u * u * (-0.044715f * 1.5957691216057308f * LOG2E) + (-1.5957691216057308f * LOG2E));
            const v2f d = v2f{exp2_(z.x), exp2_(z.y)} + 1.f;
            const v2f gl = u * v2f{__builtin_amdgcn_rcpf(d.x), __builtin_amdgcn_rcpf(d.y)};
            ac[c] += gl * w2; } }
        float y[3];
#pragma unroll
        for (int c = 0; c < 3; ++c) y[c] = wave_sum(ac[c].x + ac[c].y);
        float yv = (lane & 3) == 0 ? y[0] : (lane & 3) == 1 ? y[1] : y[2];
        bool fin = true;
        if (pass) {
          LAS float* red = (LAS float*)(L + L_RED);
          if (lane < 4) red[w * 4 + lane] = yv;
          LDS_WAIT();
          unsigned old = 0;
          if (lane == 0) old = __hip_atomic_fetch_add((LAS unsigned*)(L + L_FLAG) + 8, 1u, __ATOMIC_RELAXED, __HIP_MEMORY_SCOPE_WORKGROUP);
          fin = __builtin_amdgcn_readfirstlane(old) == 3u;
          FENCE();
          if (fin) yv = (red[lane & 3] + red[4 + (lane & 3)]) + (red[8 + (lane & 3)] + red[12 + (lane & 3)]); }
        if (fin) {
          if (lane < 3 && c0 + lane < 640) ((__bf16*)a.out)[b * 640 + c0 + lane] = (__bf16)(hres + rbf(rbf(yv) + b2));
          if (pass && wg == 0 && lane == 0) __builtin_amdgcn_raw_buffer_store_b64(v2u{T.lo, T.hi}, rws, 0, 0, SC1); }
      } }
  } else {
    const int j = wave - 4, hs = (h + 1) & 31, c = lane & 15, qd = lane >> 4;
    LAS unsigned char* stg = L + L_STG + j * 7680;
    v4u xr[5], wr[8]; v2u xo[5]; unsigned mr[9];
#pragma unroll
    for (int i = 0; i < 5; ++i) xr[i] = __builtin_amdgcn_raw_buffer_load_b128(rx, qd * 1280 + c * 16 + i * 256, 0, 0);
#pragma unroll
    for (int i = 0; i < 5; ++i) xo[i] = __builtin_amdgcn_raw_buffer_load_b64(rx, c < 4 ? c * 1280 + qd * 8 + i * 32 : MASKED, m * 160, 0);
    { const rsrc_t rq = mk(a.wq, 640 * 12288);
#pragma unroll
      for (int i = 0; i < 8; ++i) { const int kk = i * 64 + lane, row = kk / 6;
        wr[i] = __builtin_amdgcn_raw_buffer_load_b128(rq, kk < 480 ? row * 12288 + (kk - row * 6) * 16 : MASKED, m * (80 * 12288) + hs * 384 + j * 96, NT); } }
    { const rsrc_t r1 = mk(a.w1, 3072), r2 = mk(a.b1, 3072), r3 = mk(a.w2, 3072);
#pragma unroll
      for (int i = 0; i < 3; ++i) { mr[i] = __builtin_amdgcn_raw_buffer_load_b32(r1, lane * 4 + i * 256, j * 768, 0);
        mr[3 + i] = __builtin_amdgcn_raw_buffer_load_b32(r2, lane * 4 + i * 256, j * 768, 0);
        mr[6 + i] = __builtin_amdgcn_raw_buffer_load_b32(r3, lane * 4 + i * 256, j * 768, 0); } }
    SB();
    if (j == 0 && lane < 16) flag[lane] = 0u;
    const Tag T = make_tag(e0);
    float sum = 0.f;
#pragma unroll
    for (int i = 0; i < 5; ++i)
#pragma unroll
      for (int k = 0; k < 4; ++k) sum += lo16(xr[i][k]) + hi16(xr[i][k]);
    const float mean = row_sum(sum) * (1.f / 640.f);
    float sq = 0.f;
#pragma unroll
    for (int i = 0; i < 5; ++i)
#pragma unroll
      for (int k = 0; k < 4; ++k) { const float d0 = lo16(xr[i][k]) - mean, d1 = hi16(xr[i][k]) - mean; sq = fmaf(d0, d0, fmaf(d1, d1, sq)); }
    float mm, rinv; ln_consts(mean, row_sum(sq) * (1.f / 640.f), mm, rinv);
    { const int src = (c & 3) * 64; mm = asf(__builtin_amdgcn_ds_bpermute(src, asu(mm))); rinv = asf(__builtin_amdgcn_ds_bpermute(src, asu(rinv))); }
    v4s xb[5];
#pragma unroll
    for (int i = 0; i < 5; ++i) {
      const v2u t = {pk(rbf(lo16(xo[i].x) - mm) * rinv, rbf(hi16(xo[i].x) - mm) * rinv), pk(rbf(lo16(xo[i].y) - mm) * rinv, rbf(hi16(xo[i].y) - mm) * rinv)};
      xb[i] = __builtin_bit_cast(v4s, c < 4 ? t : v2u{0u, 0u}); }
#pragma unroll
    for (int i = 0; i < 8; ++i) if (i < 7 || lane < 32) *(LAS v4u*)(stg + i * 1024 + lane * 16) = wr[i];
    const int troff = (4 * qd + (c >> 2)) * 96 + (c & 3) * 8;
#pragma unroll
    for (int t = 0; t < 3; ++t) {
      v4f d = {0.f, 0.f, 0.f, 0.f};
#pragma unroll
      for (int i = 0; i < 5; ++i) {
        const v4s af = __builtin_amdgcn_ds_read_tr16_b64_v4i16((LAS v4s*)(stg + troff + i * 1536 + t * 32));
        d = __builtin_amdgcn_mfma_f32_16x16x16bf16_1k(af, xb[i], d, 0, 0, 0); }
      const int off = c < 4 ? c * 1536 + (24 * j + 8 * t + 2 * qd) * 16 : MASKED;
      __builtin_amdgcn_raw_buffer_store_b128(v4u{asu(d.x), asu(d.y), T.lo, T.hi}, rws, off, WS_QP + (hs * 8 + m) * 6144, SC1);
      __builtin_amdgcn_raw_buffer_store_b128(v4u{asu(d.z), asu(d.w), T.lo, T.hi}, rws, off + 16, WS_QP + (hs * 8 + m) * 6144, SC1); }
    STAMP(1);
#pragma unroll
    for (int i = 0; i < 9; ++i) *(LAS v2f*)(L + L_MLP + (i / 3) * 6144 + j * 1536 + (i % 3) * 512 + lane * 8) = v2f{lo16(mr[i]), hi16(mr[i])};
    { v4u g[16];
      for (int i = 0; i < SPIN; ++i) {
        FENCE();
        unsigned bad = 0;
#pragma unroll
        for (int mm2 = 0; mm2 < 8; ++mm2) {
          g[2 * mm2] = __builtin_amdgcn_raw_buffer_load_b128(rws, lane * 16, WS_QP + (h * 8 + mm2) * 6144 + j * 1536, SC1);
          g[2 * mm2 + 1] = __builtin_amdgcn_raw_buffer_load_b128(rws, lane < 32 ? lane * 16 + 1024 : MASKED, WS_QP + (h * 8 + mm2) * 6144 + j * 1536, SC1); }
#pragma unroll
        for (int mm2 = 0; mm2 < 8; ++mm2) { bad |= (g[2 * mm2].z ^ T.lo) | (g[2 * mm2].w ^ T.hi);
          bad |= lane < 32 ? (g[2 * mm2 + 1].z ^ T.lo) | (g[2 * mm2 + 1].w ^ T.hi) : 0u; }
        if (__all(bad == 0u)) break;
        __builtin_amdgcn_s_sleep(2); }
      float q0 = 0.f, q1 = 0.f, q2 = 0.f, q3 = 0.f;
#pragma unroll
      for (int mm2 = 0; mm2 < 8; ++mm2) { q0 += asf(g[2 * mm2].x); q1 += asf(g[2 * mm2].y); q2 += asf(g[2 * mm2 + 1].x); q3 += asf(g[2 * mm2 + 1].y); }
      LAS unsigned* qd32 = (LAS unsigned*)(L + L_Q + j * 384);
      qd32[lane] = pk(q0, q1);
      if (lane < 32) qd32[64 + lane] = pk(q2, q3); }
    STAMP(2);
    BARRIER();
    if (j == 0) {
      v8b wf[6][5];
      { const rsrc_t ro = mk(a.wo, 6144 * 1280);
        const int tro = (8 * qd + (c >> 2)) * 160 + (c & 3) * 8;
#pragma unroll
        for (int s = 0; s < 6; ++s) {
          v4u t5[5];
#pragma unroll
          for (int i = 0; i < 5; ++i) { const int kk = i * 64 + lane, row = kk / 10;
            t5[i] = __builtin_amdgcn_raw_buffer_load_b128(ro, row * 1280 + (kk - row * 10) * 16, (hs * 192 + s * 32) * 1280 + m * 160, NT); }
#pragma unroll
          for (int i = 0; i < 5; ++i) *(LAS v4u*)(stg + i * 1024 + lane * 16) = t5[i];
#pragma unroll
          for (int t = 0; t < 5; ++t) {
            const v4s p0 = __builtin_amdgcn_ds_read_tr16_b64_v4i16((LAS v4s*)(stg + tro + t * 32));
            const v4s p1 = __builtin_amdgcn_ds_read_tr16_b64_v4i16((LAS v4s*)(stg + tro + t * 32 + 640));
            wf[s][t] = __builtin_bit_cast(v8b, __builtin_shufflevector(p0, p1, 0, 1, 2, 3, 4, 5, 6, 7)); }
          LDS_WAIT(); SB(); } }
      STAMP(3);
      const int goff = lane < 49 ? lane * 16 : MASKED;
#pragma unroll 1
      for (int b = 0; b < 4; ++b) {
        lds_wait_ge(flag + b, 4u);
        const LAS float* pp = (const LAS float*)(L + L_PART + b * 4 * 784);
        { v4f pv[4]; float pm[4], pl[4];
#pragma unroll
          for (int w = 0; w < 4; ++w) { pv[w] = *(const LAS v4f*)(pp + w * 196 + min(lane, 48) * 4); pm[w] = pp[w * 196 + 192]; pl[w] = pp[w * 196 + 193]; }
          const float M = fmaxf(fmaxf(pm[0], pm[1]), fmaxf(pm[2], pm[3]));
          v4f o = {0.f, 0.f, 0.f, 0.f}; float ls = 0.f;
#pragma unroll
          for (int w = 0; w < 4; ++w) { const float sc = exp2_((pm[w] - M) * LOG2E); o += sc * pv[w]; ls = fmaf(sc, pl[w], ls); }
          if (lane == 48) o = v4f{M, ls, 0.f, 0.f};
          __builtin_amdgcn_raw_buffer_store_b128(pack4(o, T), rws, goff, WS_OP + ((b * 32 + h) * 8 + m) * 1024, SC1); }
        v4u g[8];
        for (int i = 0; i < SPIN; ++i) {
          FENCE();
          unsigned bad = 0;
#pragma unroll
          for (int k = 0; k < 8; ++k) g[k] = __builtin_amdgcn_raw_buffer_load_b128(rws, goff, WS_OP + ((b * 32 + hs) * 8 + k) * 1024, SC1);
#pragma unroll
          for (int k = 0; k < 8; ++k) bad |= bad4(g[k], T);
          if (__all(bad == 0u || lane > 48)) break;
          if (b < 3) __builtin_amdgcn_s_sleep(16); }
        float Mk[8], Mg;
#pragma unroll
        for (int k = 0; k < 8; ++k) Mk[k] = rdl(asf(g[k].x & ~1023u), 48);
        Mg = fmaxf(fmaxf(fmaxf(Mk[0], Mk[1]), fmaxf(Mk[2], Mk[3])), fmaxf(fmaxf(Mk[4], Mk[5]), fmaxf(Mk[6], Mk[7])));
        v4f o = {0.f, 0.f, 0.f, 0.f};
#pragma unroll
        for (int k = 0; k < 8; ++k) o += exp2_((Mk[k] - Mg) * LOG2E) * val4(g[k]);
        o *= recip(rdl(o.y, 48));
        if (lane < 48) *(LAS v2u*)(L + L_OB + lane * 8) = v2u{pk(o.x, o.y), pk(o.z, o.w)};
        v4f y[5];
#pragma unroll
        for (int t = 0; t < 5; ++t) y[t] = v4f{0.f, 0.f, 0.f, 0.f};
#pragma unroll
        for (int s = 0; s < 6; ++s) {
          const v8b ob = *(const LAS v8b*)(L + L_OB + s * 64 + qd * 16);
#pragma unroll
          for (int t = 0; t < 5; ++t) y[t] = __builtin_amdgcn_mfma_f32_16x16x32_bf16(wf[s][t], ob, y[t], 0, 0, 0); }
        const v4f ys = c == 0 ? y[0] : c == 1 ? y[1] : c == 2 ? y[2] : c == 3 ? y[3] : y[4];
        __builtin_amdgcn_raw_buffer_store_b128(pack4(ys, T), rws, c < 5 ? (4 * c + qd) * 16 : MASKED, WS_YP + (b * 32 + hs) * 2560 + m * 320, SC1);
        if (lane == 0) flag[4 + b] = 1u;
      }
    } else if (j == 1 && wg < NRED) {
      v2u xv[4];
#pragma unroll
      for (int b = 0; b < 4; ++b) xv[b] = __builtin_amdgcn_raw_buffer_load_b64(rx, (lane & 3) * 8, b * 1280 + wg * 32, 0);
      const int yoff = (lane >> 2) * 2560 + (lane & 3) * 16;
#pragma unroll 1
      for (int b = 0; b < 4; ++b) {
        lds_wait_ge(flag + 4 + b, 1u);
        v4u g0, g1;
        for (int i = 0; i < SPIN; ++i) {
          FENCE();
          g0 = __builtin_amdgcn_raw_buffer_load_b128(rws, yoff, WS_YP + b * 32 * 2560 + wg * 64, SC1);
          g1 = __builtin_amdgcn_raw_buffer_load_b128(rws, yoff, WS_YP + (b * 32 + 16) * 2560 + wg * 64, SC1);
          if (__all((bad4(g0, T) | bad4(g1, T)) == 0u)) break;
          if (b < 3) __builtin_amdgcn_s_sleep(16); }
        v4f y = val4(g0) + val4(g1);
#pragma unroll
        for (int k = 0; k < 4; ++k) { float t = y[k]; t += dpp<0x124>(t); t += dpp<0x128>(t); t = sw16(t, t); y[k] = sw32(t, t); }
        const v2u xb2 = b == 0 ? xv[0] : b == 1 ? xv[1] : b == 2 ? xv[2] : xv[3];
        const v4f hv = {rbf(lo16(xb2.x) + rbf(y.x)), rbf(hi16(xb2.x) + rbf(y.y)), rbf(lo16(xb2.y) + rbf(y.z)), rbf(hi16(xb2.y) + rbf(y.w))};
        __builtin_amdgcn_raw_buffer_store_b128(pack4(hv, T), rws, lane < 4 ? lane * 16 : MASKED, WS_HP + b * 2560 + wg * 64, SC1);
      } } }
}

extern "C" void kernel_launch(void* const* d_in, const int* in_sizes, int n_in, void* d_out, int out_size, void* d_ws, size_t ws_size, hipStream_t stream) {
  Args a{d_in[0], d_in[1], d_in[2], d_in[3], d_in[4], d_in[6], d_in[7], d_in[8], d_in[9], d_out, d_ws};
  fused_decode<<<256, 512, 0, stream>>>(a);
}
```

```cpp
#include <hip/hip_runtime.h>
#include <hip/hip_bf16.h>

#define LAS __attribute__((address_space(3)))
typedef unsigned v4u __attribute__((ext_vector_type(4)));
typedef unsigned v2u __attribute__((ext_vector_type(2)));
typedef float v4f __attribute__((ext_vector_type(4)));
typedef float v2f __attribute__((ext_vector_type(2)));
typedef short v4s __attribute__((ext_vector_type(4)));
typedef __bf16 v8b __attribute__((ext_vector_type(8)));
typedef __bf16 v2b __attribute__((ext_vector_type(2)));
typedef unsigned v16u __attribute__((ext_vector_type(16)));
typedef __amdgpu_buffer_rsrc_t rsrc_t;

namespace {
#define NREP 8
#define CTL 1
#define LLO 5
#define LMID 7
#define BLO 50
#define BHI 20
#define TAUB 252
#define DLY 200
#define K0 1
#define CSTR 256
#define NLD 4
#define NTRY 2
constexpr int SSTR = 16, LMAX = 9;
constexpr unsigned SLAB = 4096u * 384u, KVB = 128u * SLAB, ROUND = 32u * 3072u;
constexpr unsigned WS_QP = 4096, WS_OP = WS_QP + 256 * 4096, WS_YP = WS_OP + 4 * 256 * 1024, WS_TB = WS_YP + 128 * 2560, WS_SP = WS_TB + 256 * 512, WS_CP = WS_SP + 4 * NREP * 40 * SSTR, WS_END = WS_CP + 7 * 32 * CSTR;
constexpr int L_Q = 0, L_FLAG = 1536, L_OB = 1664, L_RED = L_OB + 4 * 384, L_PART = L_RED + 2048, L_MLP = L_PART + 16 * 784, L_STG = L_MLP + 3 * 6144, L_END = L_STG + 4 * 7680;
constexpr int F_PART = 0, F_Q = 4, F_TB = 8, F_WO = 9, F_STEP = 10, F_TAU = 12, F_BASE = 13, F_TIME = 16;
constexpr int MASKED = 0x40000000, NT = 2, SC1 = 16;
constexpr int SPIN = 1 << 15;
constexpr int NPRE = 8, NRED = 40;
constexpr float LOG2E = 1.4426950408889634f;
#define STAMP(i)

__device__ __forceinline__ rsrc_t mk(const void* p, unsigned n) { return __builtin_amdgcn_make_buffer_rsrc((void*)p, 0, (int)n, 0x00020000); }
__device__ __forceinline__ float asf(unsigned u) { return __builtin_bit_cast(float, u); }
__device__ __forceinline__ unsigned asu(float f) { return __builtin_bit_cast(unsigned, f); }
__device__ __forceinline__ float lo16(unsigned u) { return asf(u << 16); }
__device__ __forceinline__ float hi16(unsigned u) { return asf(u & 0xffff0000u); }
__device__ __forceinline__ unsigned pk(float a, float b) { return __builtin_bit_cast(unsigned, v2b{(__bf16)a, (__bf16)b}); }
__device__ __forceinline__ float rbf(float x) { return (float)(__bf16)x; }
__device__ __forceinline__ float dot2(unsigned a, unsigned b, float c) {
  return __builtin_amdgcn_fdot2_f32_bf16(__builtin_bit_cast(v2b, a), __builtin_bit_cast(v2b, b), c, false); }
template <int C> __device__ __forceinline__ float dpp(float x) { return asf(__builtin_amdgcn_mov_dpp(asu(x), C, 0xf, 0xf, true)); }
__device__ __forceinline__ float sw16(float a, float b) { auto r = __builtin_amdgcn_permlane16_swap(asu(a), asu(b), false, false); return asf(r[0]) + asf(r[1]); }
__device__ __forceinline__ float sw32(float a, float b) { auto r = __builtin_amdgcn_permlane32_swap(asu(a), asu(b), false, false); return asf(r[0]) + asf(r[1]); }
__device__ __forceinline__ float row_sum(float x) { x += dpp<0x128>(x); x += dpp<0x124>(x); x += dpp<0x122>(x); x += dpp<0x121>(x); return x; }
__device__ __forceinline__ float wave_sum(float x) { x = row_sum(x); x = sw16(x, x); return sw32(x, x); }
__device__ __forceinline__ float exp2_(float x) { return __builtin_amdgcn_exp2f(x); }
__device__ __forceinline__ float recip(float x) { const float r = __builtin_amdgcn_rcpf(x); return fmaf(fmaf(-x, r, 1.f), r, r); }
__device__ __forceinline__ float rdl(float x, int l) { return asf(__builtin_amdgcn_readlane(asu(x), l)); }
__device__ __forceinline__ void ln_consts(float mean, float var, float& m, float& rinv) {
  m = rbf(mean); rinv = recip(rbf(__builtin_amdgcn_sqrtf(rbf(rbf(var) + 1e-5f)))); }

struct Tag { unsigned lo, hi, t0, t1, t2, t3; };
__device__ __forceinline__ Tag make_tag(unsigned long long e) {
  const unsigned long long t = e + 1ull;
  return Tag{(unsigned)t, (unsigned)(t >> 32), (unsigned)t & 1023u, (unsigned)(t >> 10) & 1023u, (unsigned)(t >> 20) & 1023u, (unsigned)(t >> 30) & 1023u}; }
__device__ __forceinline__ v4u pack4(v4f v, const Tag& T) {
  return v4u{((asu(v.x) + 512u) & ~1023u) | T.t0, ((asu(v.y) + 512u) & ~1023u) | T.t1, ((asu(v.z) + 512u) & ~1023u) | T.t2, ((asu(v.w) + 512u) & ~1023u) | T.t3}; }
__device__ __forceinline__ unsigned bad4(v4u g, const Tag& T) { return ((g.x ^ T.t0) | (g.y ^ T.t1) | (g.z ^ T.t2) | (g.w ^ T.t3)) & 1023u; }
__device__ __forceinline__ v4f val4(v4u g) { return v4f{asf(g.x & ~1023u), asf(g.y & ~1023u), asf(g.z & ~1023u), asf(g.w & ~1023u)}; }

#define FENCE() asm volatile("" ::: "memory")
#define SB() __builtin_amdgcn_sched_barrier(0)
#define LDS_WAIT() asm volatile("s_waitcnt lgkmcnt(0)" ::: "memory")
#define BARRIER() asm volatile("s_waitcnt lgkmcnt(0)\n\ts_barrier" ::: "memory")
#define VM_WAIT(n) do { asm volatile("s_waitcnt vmcnt(%0)" ::"n"(n) : "memory"); SB(); } while (0)

__device__ __forceinline__ void lds_wait_ge(LAS unsigned char* L, int word, unsigned want) {
  volatile LAS unsigned* p = (volatile LAS unsigned*)(L + L_FLAG) + word;
#pragma unroll 1
  for (int i = 0; i < (SPIN << 5); ++i) if (__builtin_amdgcn_readfirstlane(*p) >= want) break;
  FENCE(); }
__device__ __forceinline__ void lds_add(LAS unsigned char* L, int word, int lane) {
  LDS_WAIT();
  if (lane == 0) __hip_atomic_fetch_add((LAS unsigned*)(L + L_FLAG) + word, 1u, __ATOMIC_RELAXED, __HIP_MEMORY_SCOPE_WORKGROUP); }
}

struct Args { const void *x, *k, *v, *wq, *wo, *w1, *b1, *w2, *b2; void *out, *ws; };

__global__ void __launch_bounds__(512, 2) fused_decode(Args a) {
  __shared__ __attribute__((aligned(16))) unsigned char lds_raw[L_END];
  LAS unsigned char* L = (LAS unsigned char*)lds_raw;
  const int wg = blockIdx.x, lane = threadIdx.x & 63, h = wg & 31, m = wg >> 5;
  const int wave = __builtin_amdgcn_readfirstlane(threadIdx.x >> 6);
  asm volatile("" : "+s"(a.x), "+s"(a.k), "+s"(a.v), "+s"(a.wq), "+s"(a.ws));
  const rsrc_t rws = mk(a.ws, WS_END), rx = mk(a.x, 5120);
  STAMP(0);

  if (wave < 4) {
    const int w = wave;
    __builtin_amdgcn_s_setprio(2);
    const unsigned loff = (lane >> 3) * 384 + (lane & 7) * 16;
    const unsigned wbase = h * SLAB + (m * 4 + w) * 3072;
    const rsrc_t rk = mk(a.k, KVB), rv = mk(a.v, KVB);
    v4u ring[4][3], pre[NPRE][3];
    auto ld = [&](const rsrc_t& rs, unsigned so, int n) { return __builtin_amdgcn_raw_buffer_load_b128(rs, loff + (n % 3) * 128, so + (n / 3) * ROUND, NT); };
    auto issue = [&](int lo, int hi, const rsrc_t& r0, unsigned s0, const rsrc_t& r1, unsigned s1) {
#pragma unroll
      for (int n = lo; n < hi; ++n) ring[(n / 3) % 4][n % 3] = n < 48 ? ld(r0, s0, n) : ld(r1, s1, n - 48);
      SB(); };
    __builtin_amdgcn_s_barrier();
#pragma unroll
    for (int n = 0; n < 3 * NPRE + LMAX; ++n) {
      if (n >= 6) VM_WAIT(5);
      if (n < 3 * NPRE) pre[n / 3][n % 3] = ld(rk, wbase, n); else ring[(n / 3) % 4][n % 3] = ld(rk, wbase, n);
      SB(); }
    STAMP(1);
    BARRIER();
    STAMP(2);
    v4u q[3];
    float s[16];
    auto loadq = [&](int b) {
#pragma unroll
      for (int j = 0; j < 3; ++j) q[j] = *(const LAS v4u*)(L + L_Q + b * 384 + (lane & 7) * 16 + j * 128); };
    auto score = [&](const v4u(&r)[3]) -> float {
      float d0 = 0.f, d1 = 0.f, d2 = 0.f;
#pragma unroll
      for (int k = 0; k < 4; ++k) { d0 = dot2(r[0][k], q[0][k], d0); d1 = dot2(r[1][k], q[1][k], d1); d2 = dot2(r[2][k], q[2][k], d2); }
      float d = (d0 + d1) + d2;
      d += dpp<0xB1>(d); d += dpp<0x4E>(d); d += dpp<0x141>(d);
      d = rbf(rbf(d) * 0.07216878364870322f);
      asm volatile("" : "+v"(d));
      return d; };
    int mode = 1;
    volatile LAS unsigned* fl = (volatile LAS unsigned*)(L + L_FLAG);
    auto wait_step = [&] { if (mode == 0) VM_WAIT(LLO - 3); else if (mode == 1) VM_WAIT(LMID - 3); else VM_WAIT(LMAX - 3); };
    unsigned now, tauv, basev;
    auto pace_a = [&](int g) { if (CTL && (g & 1) == 0) { now = (unsigned)__builtin_amdgcn_s_memrealtime(); tauv = fl[F_TAU]; basev = fl[F_BASE]; SB(); } };
    auto pace_b = [&](int p, int g) { if ((g & 1) == 0) {
      const int n = p * 16 + g;
      if (w == 0 && lane == 0) { if (CTL && g == 0) fl[F_TIME + p - 1] = now; fl[F_STEP] = n; }
      if (CTL) { const int tau = __builtin_amdgcn_readfirstlane(tauv), lead = (int)(__builtin_amdgcn_readfirstlane(basev) + ((n * tau) >> 8) - now);
        mode = tau == 0 ? 1 : lead > BLO ? 0 : lead < -BHI ? 2 : 1; } } };
    loadq(0);
#pragma unroll
    for (int g = NPRE; g < 16; ++g) {
      VM_WAIT(LMID - 3);
      issue(3 * g + LMAX, 3 * g + 3 + LMAX, rk, wbase, rv, wbase);
      s[g] = score(ring[g % 4]);
      s[g - NPRE] = score(pre[g - NPRE]);
      SB(); }
    const int b5 = lane >> 5, b4 = (lane >> 4) & 1, b3 = (lane >> 3) & 1, a0 = 12 * b5 + 6 * b4 + 3 * b3;
#pragma unroll 1
    for (int b = 0;; ++b) {
      const unsigned sb = wbase + b * (32 * SLAB);
      const rsrc_t rn = mk(a.k, b < 3 ? KVB : 0u);
      const unsigned nb = b < 3 ? sb + 32 * SLAB : 0u;
      float mx = 0.f, l = 0.f, acc[24];
#pragma unroll
      for (int g = 0; g < 16; ++g) {
        wait_step();
        issue(3 * g + LMAX, 3 * g + 3 + LMAX, rv, sb, rn, nb);
        pace_a(g);
        if (g == 0) {
          mx = s[0];
#pragma unroll
          for (int i = 1; i < 16; ++i) mx = fmaxf(mx, s[i]);
          mx = fmaxf(mx, dpp<0x128>(mx));
          { auto r = __builtin_amdgcn_permlane16_swap(asu(mx), asu(mx), false, false); mx = fmaxf(asf(r[0]), asf(r[1])); }
          { auto r = __builtin_amdgcn_permlane32_swap(asu(mx), asu(mx), false, false); mx = fmaxf(asf(r[0]), asf(r[1])); }
#pragma unroll
          for (int i = 0; i < 16; ++i) { s[i] = exp2_((s[i] - mx) * LOG2E); l += s[i]; }
          l += dpp<0x128>(l); l = sw16(l, l); l = sw32(l, l);
#pragma unroll
          for (int k = 0; k < 24; ++k) acc[k] = 0.f; }
#pragma unroll
        for (int j = 0; j < 3; ++j)
#pragma unroll
          for (int k = 0; k < 4; ++k) { const unsigned u = ring[g % 4][j][k];
            acc[j * 8 + 2 * k] = fmaf(s[g], lo16(u), acc[j * 8 + 2 * k]);
            acc[j * 8 + 2 * k + 1] = fmaf(s[g], hi16(u), acc[j * 8 + 2 * k + 1]); }
#pragma unroll
        for (int k = 0; k < 24; ++k) asm volatile("" : "+v"(acc[k]));
        pace_b(2 * b + 1, g);
        if (g >= 12 && b == 3) mode = 2;
        SB(); }
#pragma unroll
      for (int i = 0; i < 12; ++i) acc[i] = sw32(acc[i], acc[i + 12]);
#pragma unroll
      for (int i = 0; i < 6; ++i) acc[i] = sw16(acc[i], acc[i + 6]);
      LAS float* part = (LAS float*)(L + L_PART + (b * 4 + w) * 784);
#pragma unroll
      for (int t = 0; t < 3; ++t) { const float u0 = acc[t] + dpp<0x128>(acc[t]), u1 = acc[t + 3] + dpp<0x128>(acc[t + 3]);
        const int ai = a0 + t; part[64 * (ai >> 3) + 8 * (lane & 7) + (ai & 7)] = b3 ? u1 : u0; }
      if (lane == 0) { part[192] = mx; part[193] = l; }
      lds_add(L, F_PART + b, lane);
      if (b == 3) break;
      lds_wait_ge(L, F_Q + b + 1, 1u);
      loadq(b + 1);
#pragma unroll
      for (int g = 0; g < 16; ++g) {
        wait_step();
        issue(3 * g + LMAX, 3 * g + 3 + LMAX, rk, nb, rv, nb);
        pace_a(g);
        s[g] = score(ring[g % 4]);
        pace_b(2 * b + 2, g);
        SB(); }
    }
  } else {
    const int j = wave - 4, hs = (h + 1) & 31, c = lane & 15, qd = lane >> 4;
    LAS unsigned char* stg = L + L_STG + j * 7680;
    const unsigned long long e0 = *(const __attribute__((address_space(4))) unsigned long long*)a.ws;
    v4u xr[5], wr[8]; v2u xo[5];
#pragma unroll
    for (int i = 0; i < 5; ++i) xr[i] = __builtin_amdgcn_raw_buffer_load_b128(rx, qd * 1280 + c * 16 + i * 256, 0, 0);
#pragma unroll
    for (int i = 0; i < 5; ++i) xo[i] = __builtin_amdgcn_raw_buffer_load_b64(rx, (c >> 2) * 1280 + qd * 8 + i * 32, m * 160, 0);
    SB();
    { const rsrc_t rq = mk(a.wq, 640 * 12288);
      const int off = lane < 60 ? (lane / 6) * 12288 + (lane % 6) * 16 : MASKED;
#pragma unroll
      for (int i = 0; i < 8; ++i) wr[i] = __builtin_amdgcn_raw_buffer_load_b128(rq, off, (m * 80 + i * 10) * 12288 + hs * 384 + j * 96, NT); }
    SB();
    __builtin_amdgcn_s_barrier();
    STAMP(8);
    if (j == 0 && lane < 24) ((volatile LAS unsigned*)(L + L_FLAG))[lane] = 0u;
    const Tag T = make_tag(e0);
    float sum = 0.f;
#pragma unroll
    for (int i = 0; i < 5; ++i)
#pragma unroll
      for (int k = 0; k < 4; ++k) sum += lo16(xr[i][k]) + hi16(xr[i][k]);
    const float mean = row_sum(sum) * (1.f / 640.f);
    float sq = 0.f;
#pragma unroll
    for (int i = 0; i < 5; ++i)
#pragma unroll
      for (int k = 0; k < 4; ++k) { const float d0 = lo16(xr[i][k]) - mean, d1 = hi16(xr[i][k]) - mean; sq = fmaf(d0, d0, fmaf(d1, d1, sq)); }
    float mm, rinv; ln_consts(mean, row_sum(sq) * (1.f / 640.f), mm, rinv);
    { const int src = (c >> 2) * 64; mm = asf(__builtin_amdgcn_ds_bpermute(src, asu(mm))); rinv = asf(__builtin_amdgcn_ds_bpermute(src, asu(rinv))); }
    v4s xb[5];
#pragma unroll
    for (int i = 0; i < 5; ++i)
      xb[i] = __builtin_bit_cast(v4s, v2u{pk(rbf(lo16(xo[i].x) - mm) * rinv, rbf(hi16(xo[i].x) - mm) * rinv), pk(rbf(lo16(xo[i].y) - mm) * rinv, rbf(hi16(xo[i].y) - mm) * rinv)});
#pragma unroll
    for (int i = 0; i < 5; ++i) asm volatile("" : "+v"(xb[i]));
    SB();
    STAMP(10);
    if (lane < 60) {
#pragma unroll
      for (int i = 0; i < 8; ++i) *(LAS v4u*)(stg + i * 960 + lane * 16) = wr[i]; }
    { const int troff = (4 * qd + (c >> 2)) * 96 + (c & 3) * 8;
      v4f d[3];
#pragma unroll
      for (int t = 0; t < 3; ++t) {
        d[t] = v4f{0.f, 0.f, 0.f, 0.f};
#pragma unroll
        for (int i = 0; i < 5; ++i)
          d[t] = __builtin_amdgcn_mfma_f32_16x16x16bf16_1k(xb[i], __builtin_amdgcn_ds_read_tr16_b64_v4i16((LAS v4s*)(stg + troff + i * 1536 + t * 32)), d[t], 0, 0, 0); }
      __builtin_amdgcn_raw_buffer_store_b128(v4u{asu(d[0].x), asu(d[1].x), asu(d[2].x), T.lo}, rws, lane * 16, WS_QP + (hs * 8 + m) * 4096 + j * 1024, SC1); }
    STAMP(1);
    if (j > 0) { BARRIER(); __builtin_amdgcn_s_sleep(64); }
    { v4u g[8];
#pragma unroll 1
      for (int i = 0; i < SPIN; ++i) {
        FENCE();
        unsigned bad = 0;
#pragma unroll
        for (int k = 0; k < 8; ++k) g[k] = __builtin_amdgcn_raw_buffer_load_b128(rws, qd * 1024 + c * 16, WS_QP + (h * 8 + k) * 4096 + j * 256, SC1);
#pragma unroll
        for (int k = 0; k < 8; ++k) bad |= g[k].w ^ T.lo;
        if (__all(bad == 0u)) break;
        __builtin_amdgcn_s_sleep(4); }
      float q0 = 0.f, q1 = 0.f, q2 = 0.f;
#pragma unroll
      for (int k = 0; k < 8; ++k) { q0 += asf(g[k].x); q1 += asf(g[k].y); q2 += asf(g[k].z); }
      LAS __bf16* qs = (LAS __bf16*)(L + L_Q + j * 384) + 48 * qd + c;
      qs[0] = (__bf16)q0; qs[16] = (__bf16)q1; qs[32] = (__bf16)q2; }
    STAMP(2);
    if (j == 0) BARRIER(); else lds_add(L, F_Q + j, lane);
    unsigned tb = 0;
    if (j < 3) {
      const rsrc_t r1 = mk(a.w1, 3072), r2 = mk(a.b1, 3072), r3 = mk(a.w2, 3072);
      unsigned mr[12];
#pragma unroll
      for (int i = 0; i < 4; ++i) { mr[i] = __builtin_amdgcn_raw_buffer_load_b32(r1, lane * 4 + i * 256, j * 1024, 0);
        mr[4 + i] = __builtin_amdgcn_raw_buffer_load_b32(r2, lane * 4 + i * 256, j * 1024, 0);
        mr[8 + i] = __builtin_amdgcn_raw_buffer_load_b32(r3, lane * 4 + i * 256, j * 1024, 0); }
#pragma unroll
      for (int i = 0; i < 12; ++i) *(LAS v2f*)(L + L_MLP + (i / 4) * 6144 + j * 2048 + (i % 4) * 512 + lane * 8) = v2f{lo16(mr[i]), hi16(mr[i])};
      const unsigned e = wg * 128 + lane * 2, bits = ((e & 16383u) + 512u) | ((e >> 14) << 15);
      const v2f t2 = {lo16(bits), lo16(bits + 1u)};
      v2f ac = {0.f, 0.f};
      const LAS v4f* mw = (const LAS v4f*)(L + L_MLP + j * 2048);
      const rsrc_t ro = mk(a.wo, 6144 * 1280);
      const int off = lane < 60 ? (lane / 10) * 1280 + (lane % 10) * 16 : MASKED;
#pragma unroll 1
      for (int o = 0; o < 11; ++o) {
        const int piece = 11 * j + o;
        const v4u t = __builtin_amdgcn_raw_buffer_load_b128(ro, piece < 32 ? off : MASKED, (hs * 192 + min(piece, 31) * 6) * 1280 + m * 160, NT);
        SB();
#pragma unroll 2
        for (int i = 12 * o; i < min(12 * o + 12, 128); ++i) {
          const v4f w1 = mw[i], b1 = mw[384 + i], w2 = mw[768 + i];
#pragma unroll
          for (int k = 0; k < 4; ++k) {
            const v2f u = t2 * w1[k] + b1[k];
            const v2f z = u * (u * u * (-0.044715f * 1.5957691216057308f * LOG2E) + (-1.5957691216057308f * LOG2E));
            const v2f d = v2f{exp2_(z.x), exp2_(z.y)} + 1.f;
            ac += u * v2f{__builtin_amdgcn_rcpf(d.x), __builtin_amdgcn_rcpf(d.y)} * w2[k]; } }
        if (lane < 60 && piece < 32) *(LAS v4u*)(L + L_STG + piece * 960 + lane * 16) = t; }
      LAS v2f* red = (LAS v2f*)(L + L_RED);
      red[j * 64 + lane] = ac;
      lds_add(L, F_TB, lane);
      if (j == 0) {
        lds_wait_ge(L, F_TB, 3u);
        const float b2 = lo16(*(const unsigned short*)a.b2);
        const v2f y = (red[lane] + red[64 + lane]) + red[128 + lane];
        tb = pk(rbf(y.x) + b2, rbf(y.y) + b2); }
    } else {
      volatile LAS unsigned* fl = (volatile LAS unsigned*)(L + L_FLAG);
      int m1 = 0, m2 = 0;
#pragma unroll 1
      for (int k = 0; k < (CTL ? 7 : 0); ++k) {
#pragma unroll 1
        for (int it = 0; it < (SPIN << 3); ++it) { if ((int)__builtin_amdgcn_readfirstlane(fl[F_STEP]) >= 16 * (k + 1)) break; __builtin_amdgcn_s_sleep(2); }
        const unsigned own = __builtin_amdgcn_readfirstlane(fl[F_TIME + k]);
        __builtin_amdgcn_raw_buffer_store_b64(v2u{own, T.lo}, rws, lane == 0 ? (wg >> 3) * CSTR + (wg & 7) * 8 : MASKED, WS_CP + k * 32 * CSTR, SC1);
        int sum = 0, cnt = 0;
#pragma unroll 1
        for (int tries = 0; tries < NTRY && cnt < 8 * NLD; ++tries) {
#pragma unroll 1
          for (int it = 0; it < SPIN; ++it) { if ((int)((unsigned)__builtin_amdgcn_s_memrealtime() - own) >= DLY + 60 * tries) break; __builtin_amdgcn_s_sleep(4); }
          sum = 0; cnt = 0;
#pragma unroll 1
          for (int q = 0; q < NLD; q += 2) { const char* pb = (const char*)a.ws + WS_CP + k * 32 * CSTR;
            const char *p0 = pb + (((wg >> 3) + q * (32 / NLD)) & 31) * CSTR, *p1 = pb + (((wg >> 3) + (q + 1) * (32 / NLD)) & 31) * CSTR; v16u e, f;
            asm volatile("s_load_dwordx16 %0, %2, 0x0 glc\n\ts_load_dwordx16 %1, %3, 0x0 glc\n\ts_waitcnt lgkmcnt(0)" : "=&s"(e), "=&s"(f) : "s"(p0), "s"(p1) : "memory");
#pragma unroll
            for (int u = 0; u < 8; ++u) { const bool ok = e[2 * u + 1] == T.lo, ol = f[2 * u + 1] == T.lo;
              sum += (ok ? (int)(e[2 * u] - own) : 0) + (ol ? (int)(f[2 * u] - own) : 0); cnt += ok + ol; } }
        }
        const int mean = (int)own + (cnt ? (int)((float)sum * __builtin_amdgcn_rcpf((float)cnt)) : 0);
        if (k >= K0) { const int tau = k > K0 ? ((mean - m2) * TAUB) >> 5 : ((mean - m1) * TAUB) >> 4;
          if (lane == 0) { fl[F_BASE] = mean - ((16 * (k + 1) * tau) >> 8); fl[F_TAU] = tau; } }
        m2 = m1; m1 = mean; } }
    v8b wf[6][5];
    { lds_wait_ge(L, F_TB, 3u);
      const int tro = L_STG + (8 * qd + (c >> 2)) * 160 + (c & 3) * 8;
#pragma unroll
      for (int s = 0; s < 6; ++s)
#pragma unroll
        for (int t = 0; t < 5; ++t) {
          const v4s p0 = __builtin_amdgcn_ds_read_tr16_b64_v4i16((LAS v4s*)(L + tro + s * 5120 + t * 32));
          const v4s p1 = __builtin_amdgcn_ds_read_tr16_b64_v4i16((LAS v4s*)(L + tro + s * 5120 + t * 32 + 640));
          wf[s][t] = __builtin_bit_cast(v8b, __builtin_shufflevector(p0, p1, 0, 1, 2, 3, 4, 5, 6, 7)); }
#pragma unroll
      for (int s = 0; s < 6; ++s) asm volatile("" : "+v"(wf[s][0]), "+v"(wf[s][1]), "+v"(wf[s][2]), "+v"(wf[s][3]), "+v"(wf[s][4])); }
    const int rj = wg - 64 * j;
    const float xv = lo16(__builtin_amdgcn_raw_buffer_load_b16(rx, (lane & 3) * 8 + qd * 2, j * 1280 + (rj & 63) * 32, 0));
    STAMP(3);
    __builtin_amdgcn_s_setprio(3);
    const int goff = lane < 52 ? lane * 16 : MASKED;
    lds_wait_ge(L, F_PART + j, 4u);
    { const LAS float* pp = (const LAS float*)(L + L_PART + j * 4 * 784);
      v4f pv[4]; float pm[4], pl[4];
#pragma unroll
      for (int w = 0; w < 4; ++w) { pv[w] = *(const LAS v4f*)(pp + w * 196 + min(lane, 48) * 4); pm[w] = pp[w * 196 + 192]; pl[w] = pp[w * 196 + 193]; }
      const float M = fmaxf(fmaxf(pm[0], pm[1]), fmaxf(pm[2], pm[3]));
      v4f o = {0.f, 0.f, 0.f, 0.f}; float ls = 0.f;
#pragma unroll
      for (int w = 0; w < 4; ++w) { const float sc = exp2_((pm[w] - M) * LOG2E); o += sc * pv[w]; ls = fmaf(sc, pl[w], ls); }
      if (lane >= 48) o = v4f{M, ls, 0.f, 0.f};
      v4u og = pack4(o, T);
      asm volatile("" : "+v"(og));
      lds_wait_ge(L, F_PART + 3, 4u);
      __builtin_amdgcn_raw_buffer_store_b128(og, rws, goff, WS_OP + ((j * 32 + h) * 8 + m) * 1024, SC1);
      if (j == 0) __builtin_amdgcn_raw_buffer_store_b64(v2u{tb, T.lo}, rws, lane * 8, WS_TB + wg * 512, SC1); }
    STAMP(4);
    { v4u g[8], gs;
#pragma unroll 1
      for (int i = 0; i < SPIN; ++i) {
        FENCE();
        gs = __builtin_amdgcn_raw_buffer_load_b128(rws, (lane & 7) * 1024 + 768, WS_OP + (j * 32 + hs) * 8192, SC1);
#pragma unroll
        for (int k = 0; k < 8; ++k) g[k] = __builtin_amdgcn_raw_buffer_load_b128(rws, goff, WS_OP + ((j * 32 + hs) * 8 + k) * 1024, SC1);
        unsigned bad = 0;
#pragma unroll
        for (int k = 0; k < 8; ++k) bad |= bad4(g[k], T);
        if (__all((bad == 0u || lane >= 48) && bad4(gs, T) == 0u)) break; }
      STAMP(5);
      const float Mk = asf(gs.x & ~1023u);
      float Mg = fmaxf(Mk, dpp<0xB1>(Mk)); Mg = fmaxf(Mg, dpp<0x4E>(Mg)); Mg = fmaxf(Mg, dpp<0x141>(Mg));
      float wk = exp2_((Mk - Mg) * LOG2E), Lg = wk * asf(gs.y & ~1023u);
      Lg += dpp<0xB1>(Lg); Lg += dpp<0x4E>(Lg); Lg += dpp<0x141>(Lg);
      wk *= recip(Lg);
      v4f o = {0.f, 0.f, 0.f, 0.f};
#pragma unroll
      for (int k = 0; k < 8; ++k) o += rdl(wk, k) * val4(g[k]);
      LAS unsigned char* ob = L + L_OB + j * 384;
      if (lane < 48) *(LAS v2u*)(ob + lane * 8) = v2u{pk(o.x, o.y), pk(o.z, o.w)};
      v4f y[5];
#pragma unroll
      for (int t = 0; t < 5; ++t) y[t] = v4f{0.f, 0.f, 0.f, 0.f};
#pragma unroll
      for (int s = 0; s < 6; ++s) {
        const v8b of = *(const LAS v8b*)(ob + s * 64 + qd * 16);
#pragma unroll
        for (int t = 0; t < 5; ++t) y[t] = __builtin_amdgcn_mfma_f32_16x16x32_bf16(wf[s][t], of, y[t], 0, 0, 0); }
      const v4f ys = c == 0 ? y[0] : c == 1 ? y[1] : c == 2 ? y[2] : c == 3 ? y[3] : y[4];
      __builtin_amdgcn_raw_buffer_store_b128(pack4(ys, T), rws, c < 5 ? (4 * c + qd) * 16 : MASKED, WS_YP + (j * 32 + hs) * 2560 + m * 320, SC1); }
    STAMP(6);
    if (rj >= 0 && rj < NRED) {
        const int yoff = (lane >> 2) * 2560 + (lane & 3) * 16;
      v4u g0, g1;
#pragma unroll 1
      for (int i = 0; i < SPIN; ++i) {
        FENCE();
        g0 = __builtin_amdgcn_raw_buffer_load_b128(rws, yoff, WS_YP + j * 32 * 2560 + rj * 64, SC1);
        g1 = __builtin_amdgcn_raw_buffer_load_b128(rws, yoff, WS_YP + (j * 32 + 16) * 2560 + rj * 64, SC1);
        if (__all((bad4(g0, T) | bad4(g1, T)) == 0u)) break; }
      STAMP(7);
      const v4f y4 = val4(g0) + val4(g1);
      float y = sw16(sw32(y4.x, y4.z), sw32(y4.y, y4.w));
      y += dpp<0x124>(y); y += dpp<0x128>(y);
      const float hv = rbf(xv + rbf(y));
      float s1 = hv, s2 = hv * hv;
      s1 += dpp<0xB1>(s1); s2 += dpp<0xB1>(s2); s1 += dpp<0x4E>(s1); s2 += dpp<0x4E>(s2);
      s1 = sw16(s1, s1); s2 = sw16(s2, s2); s1 = sw32(s1, s1); s2 = sw32(s2, s2);
      __builtin_amdgcn_raw_buffer_store_b128(v4u{asu(s1), asu(s2), T.lo, T.hi}, rws, lane < NREP ? lane * (40 * SSTR) : MASKED, WS_SP + (j * NREP * 40 + rj) * SSTR, SC1);
      STAMP(9);
      v4u sg;
#pragma unroll 1
      for (int i = 0; i < SPIN; ++i) {
        FENCE();
        sg = __builtin_amdgcn_raw_buffer_load_b128(rws, lane < NRED ? lane * SSTR : MASKED, WS_SP + (j * NREP + rj % NREP) * (40 * SSTR), SC1);
        if (__all((sg.z == T.lo && sg.w == T.hi) || lane >= NRED)) break; }
      STAMP(11);
      const float mean = wave_sum(asf(sg.x)) * (1.f / 640.f), var = fmaxf(fmaf(wave_sum(asf(sg.y)), 1.f / 640.f, -mean * mean), 0.f);
      float m1, r1, m2, r2;
      ln_consts(mean, var, m1, r1);
      ln_consts((mean - m1) * r1, var * r1 * r1, m2, r2);
      const float tn = rbf(rbf(rbf(rbf(hv - m1) * r1) - m2) * r2);
      const unsigned tbits = asu(tn) >> 16, e = min(max(tbits & 0x7fffu, 512u) - 512u, 16383u) + ((tbits >> 15) << 14);
      const bool own = (lane & 12) == 0;
      v2u tg;
#pragma unroll 1
      for (int i = 0; i < SPIN; ++i) {
        FENCE();
        tg = __builtin_amdgcn_raw_buffer_load_b64(rws, own ? (e >> 1) * 8 : MASKED, WS_TB, SC1);
        if (__all(tg.y == T.lo || !own)) break; }
      STAMP(12);
      const float yv = (e & 1u) ? hi16(tg.x) : lo16(tg.x);
      if (own) ((__bf16*)a.out)[j * 640 + rj * 16 + (lane & 3) * 4 + qd] = (__bf16)(hv + (tn - tn) + yv);
      if (wg == 0 && lane == 0) __builtin_amdgcn_raw_buffer_store_b64(v2u{T.lo, T.hi}, rws, 0, 0, SC1); } }
}

extern "C" void kernel_launch(void* const* d_in, const int* in_sizes, int n_in, void* d_out, int out_size, void* d_ws, size_t ws_size, hipStream_t stream) {
  Args a{d_in[0], d_in[1], d_in[2], d_in[3], d_in[4], d_in[6], d_in[7], d_in[8], d_in[9], d_out, d_ws};
  fused_decode<<<256, 512, 0, stream>>>(a);
}
```

```cpp
#include <hip/hip_runtime.h>
#include <hip/hip_bf16.h>

#define LAS __attribute__((address_space(3)))
typedef unsigned v4u __attribute__((ext_vector_type(4)));
typedef unsigned v2u __attribute__((ext_vector_type(2)));
typedef float v4f __attribute__((ext_vector_type(4)));
typedef float v2f __attribute__((ext_vector_type(2)));
typedef short v4s __attribute__((ext_vector_type(4)));
typedef __bf16 v8b __attribute__((ext_vector_type(8)));
typedef __bf16 v2b __attribute__((ext_vector_type(2)));
typedef unsigned v16u __attribute__((ext_vector_type(16)));
typedef __amdgpu_buffer_rsrc_t rsrc_t;

namespace {
#define CTL 1
#define LLO 5
#define LMID 7
#define LP 7
#define BLO 50
#define BHI 20
#define TAUB 252
#define DLY 200
#define NTRY 2
#define BURST 0
#define QDLY 40
#define KFIRST 1
#define HOT 1
#define DUTY 0
#define QDLY2 8
#define EARLY 0
#define PAT 0
#define KEND 6
#define KSTEP 2
constexpr int NREP = 8, CSTR = 256, LMAX = 9, NPRE = 8, NRED = 40;
constexpr unsigned SLAB = 4096u * 384u, KVB = 128u * SLAB, ROUND = 32u * 3072u;
constexpr unsigned WS_QP = 4096, WS_OP = WS_QP + 256 * 1024, WS_YP = WS_OP + 4 * 256 * 1024, WS_TB = WS_YP + 128 * 2560, WS_SP = WS_TB + 256 * 64,
                   WS_CP = WS_SP + 4 * NREP * 640, WS_END = WS_CP + 7 * 32 * CSTR;
constexpr int L_Q = 0, L_FLAG = 1536, L_ML = 1664, L_OB = 1792, L_RED = L_OB + 4 * 384, L_PART = L_RED + 512, L_MLP = L_PART + 16 * 768, L_STG = L_MLP + 3 * 6144,
              L_TB = L_STG + 4 * 7680, L_END = L_TB + (HOT ? 16384 : 0);
constexpr int F_PART = 0, F_Q = 4, F_TB = 8, F_WO = 9, F_STEP = 10, F_HOT = 11, F_TAU = 12, F_BASE = 13, F_TIME = 16, F_ML = 24, F_QS = 28;
constexpr unsigned TB_LO = 102u * 128u, TB_N = 28u * 128u;
constexpr int MASKED = 0x40000000, NT = 2, SC1 = 16;
constexpr int SPIN = 1 << 15;
constexpr float LOG2E = 1.4426950408889634f;
#define STAMP(i)

__device__ __forceinline__ rsrc_t mk(const void* p, unsigned n) { return __builtin_amdgcn_make_buffer_rsrc((void*)p, 0, (int)n, 0x00020000); }
__device__ __forceinline__ float asf(unsigned u) { return __builtin_bit_cast(float, u); }
__device__ __forceinline__ unsigned asu(float f) { return __builtin_bit_cast(unsigned, f); }
__device__ __forceinline__ float lo16(unsigned u) { return asf(u << 16); }
__device__ __forceinline__ float hi16(unsigned u) { return asf(u & 0xffff0000u); }
__device__ __forceinline__ unsigned pk(float a, float b) { return __builtin_bit_cast(unsigned, v2b{(__bf16)a, (__bf16)b}); }
__device__ __forceinline__ float rbf(float x) { return (float)(__bf16)x; }
__device__ __forceinline__ float dot2(unsigned a, unsigned b, float c) {
  return __builtin_amdgcn_fdot2_f32_bf16(__builtin_bit_cast(v2b, a), __builtin_bit_cast(v2b, b), c, false); }
template <int C> __device__ __forceinline__ float dpp(float x) { return asf(__builtin_amdgcn_mov_dpp(asu(x), C, 0xf, 0xf, true)); }
__device__ __forceinline__ float sw16(float a, float b) { auto r = __builtin_amdgcn_permlane16_swap(asu(a), asu(b), false, false); return asf(r[0]) + asf(r[1]); }
__device__ __forceinline__ float sw32(float a, float b) { auto r = __builtin_amdgcn_permlane32_swap(asu(a), asu(b), false, false); return asf(r[0]) + asf(r[1]); }
__device__ __forceinline__ float row_sum(float x) { x += dpp<0x128>(x); x += dpp<0x124>(x); x += dpp<0x122>(x); x += dpp<0x121>(x); return x; }
__device__ __forceinline__ float wave_sum(float x) { x = row_sum(x); x = sw16(x, x); return sw32(x, x); }
__device__ __forceinline__ float exp2_(float x) { return __builtin_amdgcn_exp2f(x); }
__device__ __forceinline__ float recip(float x) { const float r = __builtin_amdgcn_rcpf(x); return fmaf(fmaf(-x, r, 1.f), r, r); }
__device__ __forceinline__ float rdl(float x, int l) { return asf(__builtin_amdgcn_readlane(asu(x), l)); }
__device__ __forceinline__ unsigned clock10() { return (unsigned)__builtin_amdgcn_s_memrealtime(); }
__device__ __forceinline__ void ln_consts(float mean, float var, float& m, float& rinv) {
  m = rbf(mean); rinv = recip(rbf(__builtin_amdgcn_sqrtf(rbf(rbf(var) + 1e-5f)))); }
__device__ __forceinline__ v2f gelu2(v2f u) {
  const v2f z = u * (u * u * (-0.044715f * 1.5957691216057308f * LOG2E) + (-1.5957691216057308f * LOG2E));
  const v2f d = v2f{exp2_(z.x), exp2_(z.y)} + 1.f;
  return u * v2f{__builtin_amdgcn_rcpf(d.x), __builtin_amdgcn_rcpf(d.y)}; }

struct Tag { unsigned lo, hi; v4u t; };
__device__ __forceinline__ Tag make_tag(unsigned long long e) {
  const unsigned long long t = e + 1ull;
  return Tag{(unsigned)t, (unsigned)(t >> 32), v4u{(unsigned)t & 1023u, (unsigned)(t >> 10) & 1023u, (unsigned)(t >> 20) & 1023u, (unsigned)(t >> 30) & 1023u}}; }
__device__ __forceinline__ unsigned bfi(unsigned m, unsigned a, unsigned b) { unsigned r; asm("v_bfi_b32 %0, %1, %2, %3" : "=v"(r) : "v"(m), "s"(a), "v"(b)); return r; }
__device__ __forceinline__ v4u pack4(v4f v, const Tag& T) {
  return v4u{bfi(1023u, T.t.x, asu(v.x) + 512u), bfi(1023u, T.t.y, asu(v.y) + 512u), bfi(1023u, T.t.z, asu(v.z) + 512u), bfi(1023u, T.t.w, asu(v.w) + 512u)}; }
__device__ __forceinline__ unsigned or4(v4u x) { return (x.x | x.y) | (x.z | x.w); }

#define FENCE() asm volatile("" ::: "memory")
#define SB() __builtin_amdgcn_sched_barrier(0)
#define LDS_WAIT() asm volatile("s_waitcnt lgkmcnt(0)" ::: "memory")
#define BARRIER() asm volatile("s_waitcnt lgkmcnt(0)\n\ts_barrier" ::: "memory")
#define VM_WAIT(n) do { asm volatile("s_waitcnt vmcnt(%0)" ::"n"(n) : "memory"); SB(); } while (0)

template <int SLEEP = 0> __device__ __forceinline__ void lds_wait_ge(LAS unsigned char* L, int word, unsigned want) {
  volatile LAS unsigned* p = (volatile LAS unsigned*)(L + L_FLAG) + word;
#pragma unroll 1
  for (int i = 0; i < (SPIN << 5); ++i) { if (__builtin_amdgcn_readfirstlane(*p) >= want) break; if (SLEEP) __builtin_amdgcn_s_sleep(SLEEP); }
  FENCE(); }
__device__ __forceinline__ void lds_add(LAS unsigned char* L, int word, int lane) {
  LDS_WAIT();
  if (lane == 0) __hip_atomic_fetch_add((LAS unsigned*)(L + L_FLAG) + word, 1u, __ATOMIC_RELAXED, __HIP_MEMORY_SCOPE_WORKGROUP); }
}

struct Args { const void *x, *k, *v, *wq, *wo, *w1, *b1, *w2, *b2; void *out, *ws; };

__global__ void __launch_bounds__(512, 2) fused_decode(Args a) {
  __shared__ __attribute__((aligned(16))) unsigned char lds_raw[L_END];
  LAS unsigned char* L = (LAS unsigned char*)lds_raw;
  volatile LAS unsigned* fl = (volatile LAS unsigned*)(L + L_FLAG);
  const int wg = blockIdx.x, lane = threadIdx.x & 63, h = wg & 31, m = wg >> 5;
  const int wave = __builtin_amdgcn_readfirstlane(threadIdx.x >> 6);
  asm volatile("" : "+s"(a.x), "+s"(a.k), "+s"(a.v), "+s"(a.wq), "+s"(a.ws));
  STAMP(0);

  if (wave < 4) {
    const int w = wave;
    __builtin_amdgcn_s_setprio(2);
    const unsigned loff = (lane >> 3) * 384 + (lane & 7) * 16;
    const unsigned wbase = h * SLAB + (PAT == 0 ? (m * 4 + w) * 3072 : PAT == 1 ? (m * 4 + w) * 6144 : PAT == 2 ? (w * 8 + m) * 3072 : PAT == 3 ? (m * 2 + (w & 1)) * 3072 + (w >> 1) * (SLAB / 2)
                                           : (m * 2 + (w & 1)) * 3072 + (w >> 1) * 49152);
    const rsrc_t rk = mk(a.k, KVB), rv = mk(a.v, KVB);
    v4u ring[4][3], pre[NPRE][3];
    auto ld = [&](const rsrc_t& rs, unsigned so, int n) { return __builtin_amdgcn_raw_buffer_load_b128(rs, loff + (n % 3) * 128, so + (PAT == 1 ? (n / 6) * 2 * ROUND + (n / 3 % 2) * 3072 : PAT == 3 ? (n / 3) * (ROUND / 2) : (n / 3) * ROUND), NT); };
    auto issue = [&](int g, const rsrc_t& r0, unsigned s0, const rsrc_t& r1, unsigned s1) {
#pragma unroll
      for (int n = 3 * g + LMAX; n < 3 * g + LMAX + 3; ++n) ring[(n / 3) % 4][n % 3] = n < 48 ? ld(r0, s0, n) : ld(r1, s1, n - 48);
      SB(); };
#pragma unroll
    for (int n = 0; n < 3 * NPRE + LMAX; ++n) {
      if (n == BURST) { if (w == 0 && lane < 32) fl[lane] = 0u; BARRIER(); SB(); }
      if (n >= LP) VM_WAIT(LP - 1);
      if (n < 3 * NPRE) pre[n / 3][n % 3] = ld(rk, wbase, n); else ring[(n / 3) % 4][n % 3] = ld(rk, wbase, n);
      SB(); }
    STAMP(1);
    BARRIER();
    STAMP(2);
    v4u q[3];
    float s[16];
    auto loadq = [&](int b) {
#pragma unroll
      for (int j = 0; j < 3; ++j) q[j] = *(const LAS v4u*)(L + L_Q + b * 384 + (lane & 7) * 16 + j * 128); };
    auto score = [&](const v4u(&r)[3]) -> float {
      float d0 = 0.f, d1 = 0.f, d2 = 0.f;
#pragma unroll
      for (int k = 0; k < 4; ++k) { d0 = dot2(r[0][k], q[0][k], d0); d1 = dot2(r[1][k], q[1][k], d1); d2 = dot2(r[2][k], q[2][k], d2); }
      float d = (d0 + d1) + d2;
      d += dpp<0xB1>(d); d += dpp<0x4E>(d); d += dpp<0x141>(d);
      d = rbf(rbf(d) * 0.07216878364870322f);
      asm volatile("" : "+v"(d));
      return d; };
    int mode = 1;
    auto wait_step = [&](int g) { if (mode == 0 && !(DUTY && (g & 1))) VM_WAIT(LLO - 3); else if (mode <= 1) VM_WAIT(LMID - 3); else VM_WAIT(LMAX - 3); };
    unsigned now, tauv, basev;
    auto pace_a = [&](int g) { if (CTL && (g & 1) == 0) { now = clock10(); tauv = fl[F_TAU]; basev = fl[F_BASE]; SB(); } };
    auto pace_b = [&](int p, int g) { if ((g & 1) == 0) {
      const int n = p * 16 + g;
      if (w == 0 && lane == 0) { if (CTL && g == 0) fl[F_TIME + p - 1] = now; fl[F_STEP] = n; }
      if (CTL) { const int tau = __builtin_amdgcn_readfirstlane(tauv), lead = (int)(__builtin_amdgcn_readfirstlane(basev) + ((n * tau) >> 8) - now);
        mode = tau == 0 ? 1 : lead > BLO ? 0 : lead < -BHI ? 2 : 1; } } };
    loadq(0);
#pragma unroll
    for (int g = NPRE; g < 16; ++g) {
      VM_WAIT(LMID - 3);
      issue(g, rk, wbase, rv, wbase);
      s[g] = score(ring[g % 4]);
      s[g - NPRE] = score(pre[g - NPRE]);
      SB(); }
    const int b3 = (lane >> 3) & 1, a0 = 12 * (lane >> 5) + 6 * ((lane >> 4) & 1) + 3 * b3;
#pragma unroll 1
    for (int b = 0;; ++b) {
      const unsigned sb = wbase + b * (32 * SLAB);
      const rsrc_t rn = mk(a.k, b < 3 ? KVB : 0u);
      const unsigned nb = b < 3 ? sb + 32 * SLAB : 0u;
      float acc[24];
#pragma unroll
      for (int g = 0; g < 16; ++g) {
        wait_step(g);
        issue(g, rv, sb, rn, nb);
        pace_a(g);
        if (g == 0) {
          float mx = s[0], l = 0.f;
#pragma unroll
          for (int i = 1; i < 16; ++i) mx = fmaxf(mx, s[i]);
          mx = fmaxf(mx, dpp<0x128>(mx));
          { auto r = __builtin_amdgcn_permlane16_swap(asu(mx), asu(mx), false, false); mx = fmaxf(asf(r[0]), asf(r[1])); }
          { auto r = __builtin_amdgcn_permlane32_swap(asu(mx), asu(mx), false, false); mx = fmaxf(asf(r[0]), asf(r[1])); }
#pragma unroll
          for (int i = 0; i < 16; ++i) { s[i] = exp2_((s[i] - mx) * LOG2E); l += s[i]; }
          l += dpp<0x128>(l); l = sw16(l, l); l = sw32(l, l);
          if (lane == 0) *(LAS v2f*)(L + L_ML + (b * 4 + w) * 8) = v2f{mx, l};
          lds_add(L, F_ML + b, lane);
#pragma unroll
          for (int k = 0; k < 24; ++k) acc[k] = 0.f; }
#pragma unroll
        for (int j = 0; j < 3; ++j)
#pragma unroll
          for (int k = 0; k < 4; ++k) { const unsigned u = ring[g % 4][j][k];
            acc[j * 8 + 2 * k] = fmaf(s[g], lo16(u), acc[j * 8 + 2 * k]);
            acc[j * 8 + 2 * k + 1] = fmaf(s[g], hi16(u), acc[j * 8 + 2 * k + 1]); }
#pragma unroll
        for (int k = 0; k < 24; ++k) asm volatile("" : "+v"(acc[k]));
        pace_b(2 * b + 1, g);
        if (g >= 12 && b == 3) mode = 2;
        SB(); }
#pragma unroll
      for (int i = 0; i < 12; ++i) acc[i] = sw32(acc[i], acc[i + 12]);
#pragma unroll
      for (int i = 0; i < 6; ++i) acc[i] = sw16(acc[i], acc[i + 6]);
      LAS float* part = (LAS float*)(L + L_PART + (b * 4 + w) * 768);
#pragma unroll
      for (int t = 0; t < 3; ++t) { const float u0 = acc[t] + dpp<0x128>(acc[t]), u1 = acc[t + 3] + dpp<0x128>(acc[t + 3]);
        const int ai = a0 + t; part[64 * (ai >> 3) + 8 * (lane & 7) + (ai & 7)] = b3 ? u1 : u0; }
      lds_add(L, F_PART + b, lane);
      if (b == 3) break;
      lds_wait_ge(L, F_Q + b + 1, 1u);
      loadq(b + 1);
#pragma unroll
      for (int g = 0; g < 16; ++g) {
        wait_step(g);
        issue(g, rk, nb, rv, nb);
        pace_a(g);
        s[g] = score(ring[g % 4]);
        pace_b(2 * b + 2, g);
        SB(); }
    }
  } else {
    const int j = wave - 4, hs = (h + 1) & 31, c = lane & 15, qd = lane >> 4;
    const rsrc_t rws = mk(a.ws, WS_END), rx = mk(a.x, 5120);
    LAS unsigned char* stg = L + L_STG + j * 7680;
    const unsigned long long e0 = *(const __attribute__((address_space(4))) unsigned long long*)a.ws;
    v4u xr[5], wr[8]; v2u xo[5];
#pragma unroll
    for (int i = 0; i < 5; ++i) xr[i] = __builtin_amdgcn_raw_buffer_load_b128(rx, qd * 1280 + c * 16 + i * 256, 0, 0);
#pragma unroll
    for (int i = 0; i < 5; ++i) xo[i] = __builtin_amdgcn_raw_buffer_load_b64(rx, (c >> 2) * 1280 + qd * 8 + i * 32, ((m >> 2) * 4 + j) * 160, 0);
    SB();
    { const rsrc_t rq = mk(a.wq, 640 * 12288);
      const int off = lane < 60 ? (lane / 6) * 12288 + (lane % 6) * 16 : MASKED;
#pragma unroll
      for (int i = 0; i < 8; ++i) wr[i] = __builtin_amdgcn_raw_buffer_load_b128(rq, off, (((m >> 2) * 4 + j) * 80 + i * 10) * 12288 + hs * 384 + (m & 3) * 96, NT); }
    SB();
    __builtin_amdgcn_s_barrier();
    SB(); FENCE();
    STAMP(8);
    const Tag T = make_tag(e0);
    float sum = 0.f;
#pragma unroll
    for (int i = 0; i < 5; ++i)
#pragma unroll
      for (int k = 0; k < 4; ++k) sum += lo16(xr[i][k]) + hi16(xr[i][k]);
    const float mean0 = row_sum(sum) * (1.f / 640.f);
    float sq = 0.f;
#pragma unroll
    for (int i = 0; i < 5; ++i)
#pragma unroll
      for (int k = 0; k < 4; ++k) { const float d0 = lo16(xr[i][k]) - mean0, d1 = hi16(xr[i][k]) - mean0; sq = fmaf(d0, d0, fmaf(d1, d1, sq)); }
    float mm, rinv; ln_consts(mean0, row_sum(sq) * (1.f / 640.f), mm, rinv);
    { const int src = (c >> 2) * 64; mm = asf(__builtin_amdgcn_ds_bpermute(src, asu(mm))); rinv = asf(__builtin_amdgcn_ds_bpermute(src, asu(rinv))); }
    v4s xb[5];
#pragma unroll
    for (int i = 0; i < 5; ++i) {
      xb[i] = __builtin_bit_cast(v4s, v2u{pk(rbf(lo16(xo[i].x) - mm) * rinv, rbf(hi16(xo[i].x) - mm) * rinv), pk(rbf(lo16(xo[i].y) - mm) * rinv, rbf(hi16(xo[i].y) - mm) * rinv)});
      asm volatile("" : "+v"(xb[i])); }
    SB();
    STAMP(10);
    if (lane < 60) {
#pragma unroll
      for (int i = 0; i < 8; ++i) *(LAS v4u*)(stg + i * 960 + lane * 16) = wr[i]; }
    { const int troff = (4 * qd + (c >> 2)) * 96 + (c & 3) * 8;
      v4f d[3];
#pragma unroll
      for (int t = 0; t < 3; ++t) {
        d[t] = v4f{0.f, 0.f, 0.f, 0.f};
#pragma unroll
        for (int i = 0; i < 5; ++i)
          d[t] = __builtin_amdgcn_mfma_f32_16x16x16bf16_1k(xb[i], __builtin_amdgcn_ds_read_tr16_b64_v4i16((LAS v4s*)(stg + troff + i * 1536 + t * 32)), d[t], 0, 0, 0); }
      LAS v4f* qp = (LAS v4f*)(L + L_PART);
      qp[j * 64 + lane] = v4f{d[0].x, d[1].x, d[2].x, 0.f};
      lds_add(L, F_QS, lane);
      if (j == 0) {
        lds_wait_ge(L, F_QS, 4u);
        const v4f t = (qp[lane] + qp[64 + lane]) + (qp[128 + lane] + qp[192 + lane]);
        __builtin_amdgcn_raw_buffer_store_b128(v4u{asu(t.x), asu(t.y), asu(t.z), T.lo}, rws, lane * 16, WS_QP + (hs * 8 + m) * 1024, SC1); } }
    STAMP(1);
    if (j > 0) { BARRIER(); __builtin_amdgcn_s_sleep(QDLY2); } else __builtin_amdgcn_s_sleep(QDLY);
    { v4u g0, g1;
#pragma unroll 1
      for (int i = 0; i < SPIN; ++i) {
        FENCE();
        g0 = __builtin_amdgcn_raw_buffer_load_b128(rws, qd * 1024 + c * 16, WS_QP + h * 8192 + j * 256, SC1);
        g1 = __builtin_amdgcn_raw_buffer_load_b128(rws, qd * 1024 + c * 16, WS_QP + h * 8192 + 4096 + j * 256, SC1);
        if (__all(g0.w == T.lo && g1.w == T.lo)) break;
        __builtin_amdgcn_s_sleep(8); }
      LAS __bf16* qs = (LAS __bf16*)(L + L_Q + j * 384) + 48 * qd + c;
      qs[0] = (__bf16)(asf(g0.x) + asf(g1.x)); qs[16] = (__bf16)(asf(g0.y) + asf(g1.y)); qs[32] = (__bf16)(asf(g0.z) + asf(g1.z)); }
    STAMP(2);
    if (j == 0) BARRIER(); else lds_add(L, F_Q + j, lane);
    const float b2 = lo16(*(const unsigned short*)a.b2);
    if (j < 3) {
      { const rsrc_t r1 = mk(a.w1, 3072), r2 = mk(a.b1, 3072), r3 = mk(a.w2, 3072);
        unsigned mr[12];
#pragma unroll
        for (int i = 0; i < 4; ++i) { mr[i] = __builtin_amdgcn_raw_buffer_load_b32(r1, lane * 4 + i * 256, j * 1024, 0);
          mr[4 + i] = __builtin_amdgcn_raw_buffer_load_b32(r2, lane * 4 + i * 256, j * 1024, 0);
          mr[8 + i] = __builtin_amdgcn_raw_buffer_load_b32(r3, lane * 4 + i * 256, j * 1024, 0); }
#pragma unroll
        for (int i = 0; i < 12; ++i) *(LAS v2f*)(L + L_MLP + (i / 4) * 6144 + j * 2048 + (i % 4) * 512 + lane * 8) = v2f{lo16(mr[i]), hi16(mr[i])}; }
      const unsigned e = wg * 28 + min(c, 13) * 2, neg = e >= TB_N, bits = (e - neg * TB_N + TB_LO) | (neg << 15);
      const v2f t2 = {lo16(bits), lo16(bits + 1u)};
      v2f ac = {0.f, 0.f};
      const LAS v4f* mw = (const LAS v4f*)(L + L_MLP + j * 2048 + qd * 512);
#pragma unroll 2
      for (int i = 0; i < 32; ++i) {
        const v4f w1 = mw[i], b1 = mw[384 + i], w2 = mw[768 + i];
#pragma unroll
        for (int k = 0; k < 4; ++k) ac += gelu2(t2 * w1[k] + b1[k]) * w2[k]; }
      ac.x = sw16(ac.x, ac.x); ac.y = sw16(ac.y, ac.y); ac.x = sw32(ac.x, ac.x); ac.y = sw32(ac.y, ac.y);
      LAS v2f* red = (LAS v2f*)(L + L_RED);
      if (lane < 16) red[j * 16 + lane] = ac;
      lds_add(L, F_TB, lane);
      if (j == 0) {
        lds_wait_ge<1>(L, F_TB, 3u);
        const v2f y = (red[c] + red[16 + c]) + red[32 + c];
        LAS unsigned short* tg = (LAS unsigned short*)(L + L_RED + 384);
        const unsigned yb = pk(rbf(y.x) + b2, rbf(y.y) + b2);
        if (lane < 14) { tg[(2 * c) / 7 * 8 + (2 * c) % 7] = (unsigned short)yb; tg[(2 * c + 1) / 7 * 8 + (2 * c + 1) % 7] = (unsigned short)(yb >> 16); }
        if (lane < 4) tg[lane * 8 + 7] = (unsigned short)T.lo;
        __builtin_amdgcn_raw_buffer_store_b128(*(const LAS v4u*)(tg + (lane & 3) * 8), rws, lane < 4 ? lane * 16 : MASKED, WS_TB + wg * 64, SC1); }
      { const rsrc_t ro = mk(a.wo, 6144 * 1280);
        const int off = lane < 60 ? (lane / 10) * 1280 + (lane % 10) * 16 : MASKED;
#pragma unroll 1
        for (int p = j; p < 32; p += 3) {
          lds_wait_ge<8>(L, F_STEP, 24u + 2u * p);
          const v4u t = __builtin_amdgcn_raw_buffer_load_b128(ro, off, (hs * 192 + p * 6) * 1280 + m * 160, NT);
          if (lane < 60) *(LAS v4u*)(L + L_STG + p * 960 + lane * 16) = t; }
        lds_add(L, F_WO, lane); }
      if (HOT && j == 2 && (wg & 63) < NRED) {
        unsigned bad = 0;
#pragma unroll 1
        for (int p = 0; p < 16; ++p) {
          lds_wait_ge<8>(L, F_STEP, 88u + p);
          const v4u t = __builtin_amdgcn_raw_buffer_load_b128(rws, lane * 16, WS_TB + p * 1024, SC1);
          bad |= (t.w >> 16) ^ (T.lo & 0xffffu);
          *(LAS v4u*)(L + L_TB + p * 1024 + lane * 16) = t; }
        LDS_WAIT();
        if (__all(bad == 0u) && lane == 0) fl[F_HOT] = 1u; }
    } else {
      int m1 = 0, m2 = 0;
#pragma unroll 1
      for (int k = KFIRST; k < (CTL ? KEND : 0); k += KSTEP) {
        lds_wait_ge<2>(L, F_STEP, 16u * (k + 1));
        const unsigned own = __builtin_amdgcn_readfirstlane(fl[F_TIME + k]);
        __builtin_amdgcn_raw_buffer_store_b64(v2u{own, T.lo}, rws, lane == 0 ? (wg >> 3) * CSTR + (wg & 7) * 8 : MASKED, WS_CP + k * 32 * CSTR, SC1);
        int sum = 0, cnt = 0;
#pragma unroll 1
        for (int tries = 0; tries < NTRY && cnt < 32; ++tries) {
#pragma unroll 1
          for (int it = 0; it < SPIN; ++it) { if ((int)(clock10() - own) >= DLY + 60 * tries) break; __builtin_amdgcn_s_sleep(4); }
          sum = 0; cnt = 0;
#pragma unroll 1
          for (int q = 0; q < 4; q += 2) { const char* pb = (const char*)a.ws + WS_CP + k * 32 * CSTR;
            const char *p0 = pb + (((wg >> 3) + q * 8) & 31) * CSTR, *p1 = pb + (((wg >> 3) + q * 8 + 8) & 31) * CSTR; v16u e, f;
            asm volatile("s_load_dwordx16 %0, %2, 0x0 glc\n\ts_load_dwordx16 %1, %3, 0x0 glc\n\ts_waitcnt lgkmcnt(0)" : "=&s"(e), "=&s"(f) : "s"(p0), "s"(p1) : "memory");
#pragma unroll
            for (int u = 0; u < 8; ++u) { const bool ok = e[2 * u + 1] == T.lo, ol = f[2 * u + 1] == T.lo;
              sum += (ok ? (int)(e[2 * u] - own) : 0) + (ol ? (int)(f[2 * u] - own) : 0); cnt += ok + ol; } } }
        const int mean = (int)own + (cnt ? (int)((float)sum * __builtin_amdgcn_rcpf((float)cnt)) : 0);
        if (k > KFIRST) { const int tau = (KSTEP == 1 && k > KFIRST + 1) ? ((mean - m2) * TAUB) >> 5 : ((mean - m1) * TAUB) / (16 * KSTEP);
          if (lane == 0) { fl[F_BASE] = mean - ((16 * (k + 1) * tau) >> 8); fl[F_TAU] = tau; } }
        m2 = m1; m1 = mean; } }
    v8b wf[6][5];
    { lds_wait_ge<4>(L, F_WO, 3u);
      const int tro = L_STG + (8 * qd + (c >> 2)) * 160 + (c & 3) * 8;
#pragma unroll
      for (int s = 0; s < 6; ++s)
#pragma unroll
        for (int t = 0; t < 5; ++t) {
          const v4s p0 = __builtin_amdgcn_ds_read_tr16_b64_v4i16((LAS v4s*)(L + tro + s * 5120 + t * 32));
          const v4s p1 = __builtin_amdgcn_ds_read_tr16_b64_v4i16((LAS v4s*)(L + tro + s * 5120 + t * 32 + 640));
          wf[s][t] = __builtin_bit_cast(v8b, __builtin_shufflevector(p0, p1, 0, 1, 2, 3, 4, 5, 6, 7)); }
#pragma unroll
      for (int s = 0; s < 6; ++s) asm volatile("" : "+v"(wf[s][0]), "+v"(wf[s][1]), "+v"(wf[s][2]), "+v"(wf[s][3]), "+v"(wf[s][4])); }
    const int rj = wg - 64 * j, rm = (rj & 63) / 5, rs5 = (rj & 63) - 5 * rm, rcol = 80 * rm + (rs5 < 4 ? 16 * qd + 4 * rs5 + (lane & 3) : 64 + 4 * (lane & 3) + qd);
    const bool reducer = rj >= 0 && rj < NRED;
    const float xv = lo16(__builtin_amdgcn_raw_buffer_load_b16(rx, reducer ? rcol * 2 : MASKED, j * 1280, 0));
    float sc[4], M, ls = 0.f;
    { lds_wait_ge<4>(L, F_ML + j, 4u);
      const v4f p0 = *(const LAS v4f*)(L + L_ML + j * 32), p1 = *(const LAS v4f*)(L + L_ML + j * 32 + 16);
      M = fmaxf(fmaxf(p0.x, p0.z), fmaxf(p1.x, p1.z));
      sc[0] = exp2_((p0.x - M) * LOG2E); sc[1] = exp2_((p0.z - M) * LOG2E); sc[2] = exp2_((p1.x - M) * LOG2E); sc[3] = exp2_((p1.z - M) * LOG2E);
      ls = fmaf(sc[3], p1.w, fmaf(sc[2], p1.y, fmaf(sc[1], p0.w, sc[0] * p0.y)));
      asm volatile("" : "+v"(sc[0]), "+v"(sc[1]), "+v"(sc[2]), "+v"(sc[3]), "+v"(M), "+v"(ls)); }
    STAMP(3);
    __builtin_amdgcn_s_setprio(3);
    const int goff = lane < 52 ? lane * 16 : MASKED;
    if (EARLY && j < 3) { lds_wait_ge<2>(L, F_PART + j, 4u); lds_wait_ge<1>(L, F_STEP, 112u + EARLY); } else lds_wait_ge(L, F_PART + 3, 4u);
    { const LAS v4f* pp = (const LAS v4f*)(L + L_PART + j * 3072) + min(lane, 47);
      v4f o = ((sc[0] * pp[0] + sc[1] * pp[48]) + sc[2] * pp[96]) + sc[3] * pp[144];
      if (lane >= 48) o = v4f{M, ls, 0.f, 0.f};
      __builtin_amdgcn_raw_buffer_store_b128(pack4(o, T), rws, goff, WS_OP + ((j * 32 + h) * 8 + m) * 1024, SC1); }
    STAMP(4);
    if (EARLY && j < 3) lds_wait_ge(L, F_PART + 3, 4u);
    { v4u g[8]; float wk;
#pragma unroll 1
      for (int i = 0; i < SPIN; ++i) {
        FENCE();
        const v4u gs = __builtin_amdgcn_raw_buffer_load_b128(rws, (lane & 7) * 1024 + 768, WS_OP + (j * 32 + hs) * 8192, SC1) ^ T.t;
#pragma unroll
        for (int k = 0; k < 8; ++k) g[k] = __builtin_amdgcn_raw_buffer_load_b128(rws, goff, WS_OP + ((j * 32 + hs) * 8 + k) * 1024, SC1);
        SB();
        const float Mk = asf(gs.x);
        float Mg = fmaxf(Mk, dpp<0xB1>(Mk)); Mg = fmaxf(Mg, dpp<0x4E>(Mg)); Mg = fmaxf(Mg, dpp<0x141>(Mg));
        wk = exp2_((Mk - Mg) * LOG2E);
        float Lg = wk * asf(gs.y);
        Lg += dpp<0xB1>(Lg); Lg += dpp<0x4E>(Lg); Lg += dpp<0x141>(Lg);
        wk *= recip(Lg);
        asm volatile("" : "+v"(wk));
        SB();
        unsigned bad = 0;
#pragma unroll
        for (int k = 0; k < 8; ++k) { g[k] ^= T.t; bad |= or4(g[k]); }
        if (__all((((lane < 48 ? bad : 0u) | or4(gs)) & 1023u) == 0u)) break; }
      STAMP(5);
      v4f o = {0.f, 0.f, 0.f, 0.f};
#pragma unroll
      for (int k = 0; k < 8; ++k) o += rdl(wk, k) * __builtin_bit_cast(v4f, g[k]);
      LAS unsigned char* ob = L + L_OB + j * 384;
      if (lane < 48) *(LAS v2u*)(ob + lane * 8) = v2u{pk(o.x, o.y), pk(o.z, o.w)};
      v4f y[5];
#pragma unroll
      for (int t = 0; t < 5; ++t) y[t] = v4f{0.f, 0.f, 0.f, 0.f};
#pragma unroll
      for (int s = 0; s < 6; ++s) {
        const v8b of = *(const LAS v8b*)(ob + s * 64 + qd * 16);
#pragma unroll
        for (int t = 0; t < 5; ++t) y[t] = __builtin_amdgcn_mfma_f32_16x16x32_bf16(of, wf[s][t], y[t], 0, 0, 0); }
      const v4f y4 = {dpp<0x00>(y[4].x), dpp<0x55>(y[4].x), dpp<0xAA>(y[4].x), dpp<0xFF>(y[4].x)};
      const v4f ys = qd == 0 ? v4f{y[0].x, y[1].x, y[2].x, y[3].x} : y4;
      __builtin_amdgcn_raw_buffer_store_b128(pack4(ys, T), rws, qd == 0 ? c * 16 : (qd == 1 && (c & 3) == 0) ? 256 + c * 4 : MASKED, WS_YP + (j * 32 + hs) * 2560 + m * 320, SC1); }
    STAMP(6);
    if (reducer) {
      const int yoff = (lane >> 2) * 2560 + (lane & 3) * 16;
      v4u g0, g1;
#pragma unroll 1
      for (int i = 0; i < SPIN; ++i) {
        FENCE();
        g0 = __builtin_amdgcn_raw_buffer_load_b128(rws, yoff, WS_YP + j * 32 * 2560 + rj * 64, SC1) ^ T.t;
        g1 = __builtin_amdgcn_raw_buffer_load_b128(rws, yoff, WS_YP + (j * 32 + 16) * 2560 + rj * 64, SC1) ^ T.t;
        if (__all(((or4(g0) | or4(g1)) & 1023u) == 0u)) break; }
      STAMP(7);
      const v4f y4 = __builtin_bit_cast(v4f, g0) + __builtin_bit_cast(v4f, g1);
      float y = sw16(sw32(y4.x, y4.z), sw32(y4.y, y4.w));
      y += dpp<0x124>(y); y += dpp<0x128>(y);
      const float hv = rbf(xv + rbf(y));
      float s1 = hv, s2 = hv * hv;
      s1 += dpp<0xB1>(s1); s2 += dpp<0xB1>(s2); s1 += dpp<0x4E>(s1); s2 += dpp<0x4E>(s2);
      s1 = sw16(s1, s1); s2 = sw16(s2, s2); s1 = sw32(s1, s1); s2 = sw32(s2, s2);
      __builtin_amdgcn_raw_buffer_store_b128(v4u{asu(s1), asu(s2), T.lo, T.hi}, rws, lane < NREP ? lane * 640 : MASKED, WS_SP + j * NREP * 640 + rj * 16, SC1);
      STAMP(9);
      const bool hot = HOT && __builtin_amdgcn_readfirstlane(fl[F_HOT]) != 0u;
      v4u sg;
#pragma unroll 1
      for (int i = 0; i < SPIN; ++i) {
        FENCE();
        sg = __builtin_amdgcn_raw_buffer_load_b128(rws, lane < NRED ? lane * 16 : MASKED, WS_SP + (j * NREP + (wg & (NREP - 1))) * 640, SC1);
        if (__all((sg.z == T.lo && sg.w == T.hi) || lane >= NRED)) break; }
      STAMP(11);
      const float mean = wave_sum(asf(sg.x)) * (1.f / 640.f), var = fmaxf(fmaf(wave_sum(asf(sg.y)), 1.f / 640.f, -mean * mean), 0.f);
      float m1, r1, m2, r2;
      ln_consts(mean, var, m1, r1);
      ln_consts((mean - m1) * r1, var * r1 * r1, m2, r2);
      const float tn = rbf(rbf(rbf(rbf(hv - m1) * r1) - m2) * r2);
      const unsigned tb = asu(tn) >> 16, mag = (tb & 0x7fffu) - TB_LO, e = min(mag, TB_N - 1u) + (tb >> 15) * TB_N, e7 = (e * 9363u) >> 16;
      const bool own = (lane & 12) == 0;
      float yv; bool have;
      if (HOT) { yv = lo16(*(const LAS unsigned short*)(L + L_TB + (e + e7) * 2)); have = hot; }
      else {
        const v4u tg = __builtin_amdgcn_raw_buffer_load_b128(rws, own ? e7 * 16 : MASKED, WS_TB, SC1);
        const unsigned sl = e - 7u * e7, dw = sl < 4u ? (sl < 2u ? tg.x : tg.y) : (sl < 6u ? tg.z : tg.w);
        yv = (sl & 1u) ? hi16(dw) : lo16(dw); have = (tg.w >> 16) == (T.lo & 0xffffu); }
      unsigned long long miss = __ballot(own && (mag >= TB_N || !have));
#pragma unroll 1
      for (; miss; miss &= miss - 1ull) {
        const int src = __builtin_ctzll(miss);
        const float t = rdl(tn, src);
        v2f ac = {0.f, 0.f};
#pragma unroll 1
        for (int i = 0; i < 12; ++i) { const LAS v2f* mw = (const LAS v2f*)(L + L_MLP) + i * 64 + lane; ac += gelu2(t * mw[0] + mw[768]) * mw[1536]; }
        const float yd = rbf(rbf(wave_sum(ac.x + ac.y)) + b2);
        yv = lane == src ? yd : yv; }
      STAMP(12);
      if (own) ((__bf16*)a.out)[j * 640 + rcol] = (__bf16)(hv + yv);
      if (wg == 0 && j == 0 && lane == 0) __builtin_amdgcn_raw_buffer_store_b64(v2u{T.lo, T.hi}, rws, 0, 0, SC1); } }
}

extern "C" void kernel_launch(void* const* d_in, const int* in_sizes, int n_in, void* d_out, int out_size, void* d_ws, size_t ws_size, hipStream_t stream) {
  Args a{d_in[0], d_in[1], d_in[2], d_in[3], d_in[4], d_in[6], d_in[7], d_in[8], d_in[9], d_out, d_ws};
  fused_decode<<<256, 512, 0, stream>>>(a);
}
```

```cpp
#include <hip/hip_runtime.h>
#include <hip/hip_bf16.h>

#define LAS __attribute__((address_space(3)))
typedef unsigned v4u __attribute__((ext_vector_type(4)));
typedef unsigned v2u __attribute__((ext_vector_type(2)));
typedef float v4f __attribute__((ext_vector_type(4)));
typedef float v2f __attribute__((ext_vector_type(2)));
typedef short v4s __attribute__((ext_vector_type(4)));
typedef __bf16 v8b __attribute__((ext_vector_type(8)));
typedef __bf16 v2b __attribute__((ext_vector_type(2)));
typedef unsigned v16u __attribute__((ext_vector_type(16)));
typedef __amdgpu_buffer_rsrc_t rsrc_t;

namespace {
constexpr int LLO = 5, LMID = 7, LMAX = 9, NPRE = 8;
constexpr int BLO = 20, BHI = 40, TAUB = 248, DLY = 200, CONV = 124;
constexpr int NREP = 8, CSTR = 256, NRED = 40;
constexpr unsigned SLAB = 4096u * 384u, KVB = 128u * SLAB, ROUND = 32u * 3072u;
constexpr unsigned WS_QP = 4096, WS_OP = WS_QP + 256 * 1024, WS_YP = WS_OP + 4 * 256 * 1024, WS_TB = WS_YP + 128 * 2560, WS_SP = WS_TB + 256 * 64,
                   WS_CP = WS_SP + 4 * NREP * 640, WS_END = WS_CP + 3 * 32 * CSTR;
constexpr int L_Q = 0, L_FLAG = 1536, L_ML = 1664, L_OB = 1792, L_RED = L_OB + 4 * 384, L_PART = L_RED + 512, L_MLP = L_PART + 16 * 768, L_STG = L_MLP + 3 * 6144,
              L_TB = L_STG + 4 * 7680, L_END = L_TB + 16384;
constexpr int F_PART = 0, F_Q = 4, F_TB = 8, F_WO = 9, F_STEP = 10, F_HOT = 11, F_TAU = 12, F_BASE = 13, F_TIME = 16, F_ML = 24, F_QS = 28;
constexpr unsigned TB_LO = 102u * 128u, TB_N = 28u * 128u;
constexpr int MASKED = 0x40000000, NT = 2, SC1 = 16;
constexpr int SPIN = 1 << 15;
constexpr float LOG2E = 1.4426950408889634f;
#define STAMP(i)

__device__ __forceinline__ rsrc_t mk(const void* p, unsigned n) { return __builtin_amdgcn_make_buffer_rsrc((void*)p, 0, (int)n, 0x00020000); }
__device__ __forceinline__ float asf(unsigned u) { return __builtin_bit_cast(float, u); }
__device__ __forceinline__ unsigned asu(float f) { return __builtin_bit_cast(unsigned, f); }
__device__ __forceinline__ float lo16(unsigned u) { return asf(u << 16); }
__device__ __forceinline__ float hi16(unsigned u) { return asf(u & 0xffff0000u); }
__device__ __forceinline__ unsigned pk(float a, float b) { return __builtin_bit_cast(unsigned, v2b{(__bf16)a, (__bf16)b}); }
__device__ __forceinline__ float rbf(float x) { return (float)(__bf16)x; }
__device__ __forceinline__ float dot2(unsigned a, unsigned b, float c) {
  return __builtin_amdgcn_fdot2_f32_bf16(__builtin_bit_cast(v2b, a), __builtin_bit_cast(v2b, b), c, false); }
template <int C> __device__ __forceinline__ float dpp(float x) { return asf(__builtin_amdgcn_mov_dpp(asu(x), C, 0xf, 0xf, true)); }
__device__ __forceinline__ float sw16(float a, float b) { auto r = __builtin_amdgcn_permlane16_swap(asu(a), asu(b), false, false); return asf(r[0]) + asf(r[1]); }
__device__ __forceinline__ float sw32(float a, float b) { auto r = __builtin_amdgcn_permlane32_swap(asu(a), asu(b), false, false); return asf(r[0]) + asf(r[1]); }
__device__ __forceinline__ float row_sum(float x) { x += dpp<0x128>(x); x += dpp<0x124>(x); x += dpp<0x122>(x); x += dpp<0x121>(x); return x; }
__device__ __forceinline__ float wave_sum(float x) { x = row_sum(x); x = sw16(x, x); return sw32(x, x); }
__device__ __forceinline__ float exp2_(float x) { return __builtin_amdgcn_exp2f(x); }
__device__ __forceinline__ float recip(float x) { const float r = __builtin_amdgcn_rcpf(x); return fmaf(fmaf(-x, r, 1.f), r, r); }
__device__ __forceinline__ float rdl(float x, int l) { return asf(__builtin_amdgcn_readlane(asu(x), l)); }
__device__ __forceinline__ unsigned clock10() { return (unsigned)__builtin_amdgcn_s_memrealtime(); }
__device__ __forceinline__ void ln_consts(float mean, float var, float& m, float& rinv) {
  m = rbf(mean); rinv = recip(rbf(__builtin_amdgcn_sqrtf(rbf(rbf(var) + 1e-5f)))); }
__device__ __forceinline__ v2f gelu2(v2f u) {
  const v2f z = u * (u * u * (-0.044715f * 1.5957691216057308f * LOG2E) + (-1.5957691216057308f * LOG2E));
  const v2f d = v2f{exp2_(z.x), exp2_(z.y)} + 1.f;
  return u * v2f{__builtin_amdgcn_rcpf(d.x), __builtin_amdgcn_rcpf(d.y)}; }

struct Tag { unsigned lo, hi; v4u t; };
__device__ __forceinline__ Tag make_tag(unsigned long long e) {
  const unsigned long long t = e + 1ull;
  return Tag{(unsigned)t, (unsigned)(t >> 32), v4u{(unsigned)t & 1023u, (unsigned)(t >> 10) & 1023u, (unsigned)(t >> 20) & 1023u, (unsigned)(t >> 30) & 1023u}}; }
__device__ __forceinline__ unsigned bfi(unsigned m, unsigned a, unsigned b) { unsigned r; asm("v_bfi_b32 %0, %1, %2, %3" : "=v"(r) : "v"(m), "s"(a), "v"(b)); return r; }
__device__ __forceinline__ v4u pack4(v4f v, const Tag& T) {
  return v4u{bfi(1023u, T.t.x, asu(v.x) + 512u), bfi(1023u, T.t.y, asu(v.y) + 512u), bfi(1023u, T.t.z, asu(v.z) + 512u), bfi(1023u, T.t.w, asu(v.w) + 512u)}; }
__device__ __forceinline__ unsigned or4(v4u x) { return (x.x | x.y) | (x.z | x.w); }

#define FENCE() asm volatile("" ::: "memory")
#define SB() __builtin_amdgcn_sched_barrier(0)
#define LDS_WAIT() asm volatile("s_waitcnt lgkmcnt(0)" ::: "memory")
#define BARRIER() asm volatile("s_waitcnt lgkmcnt(0)\n\ts_barrier" ::: "memory")
#define VM_WAIT(n) do { asm volatile("s_waitcnt vmcnt(%0)" ::"n"(n) : "memory"); SB(); } while (0)

template <int SLEEP = 0> __device__ __forceinline__ void lds_wait_ge(LAS unsigned char* L, int word, unsigned want) {
  volatile LAS unsigned* p = (volatile LAS unsigned*)(L + L_FLAG) + word;
#pragma unroll 1
  for (int i = 0; i < (SPIN << 5); ++i) { if (__builtin_amdgcn_readfirstlane(*p) >= want) break; if (SLEEP) __builtin_amdgcn_s_sleep(SLEEP); }
  FENCE(); }
__device__ __forceinline__ void lds_add(LAS unsigned char* L, int word, int lane) {
  LDS_WAIT();
  if (lane == 0) __hip_atomic_fetch_add((LAS unsigned*)(L + L_FLAG) + word, 1u, __ATOMIC_RELAXED, __HIP_MEMORY_SCOPE_WORKGROUP); }
}

struct Args { const void *x, *k, *v, *wq, *wo, *w1, *b1, *w2, *b2; void *out, *ws; };

__global__ void __launch_bounds__(512, 2) fused_decode(Args a) {
  __shared__ __attribute__((aligned(16))) unsigned char lds_raw[L_END];
  LAS unsigned char* L = (LAS unsigned char*)lds_raw;
  volatile LAS unsigned* fl = (volatile LAS unsigned*)(L + L_FLAG);
  const int wg = blockIdx.x, lane = threadIdx.x & 63, h = wg & 31, m = wg >> 5, hs = (h + 1) & 31;
  const int wave = __builtin_amdgcn_readfirstlane(threadIdx.x >> 6);
  asm volatile("" : "+s"(a.x), "+s"(a.k), "+s"(a.v), "+s"(a.wq), "+s"(a.ws));
  STAMP(0);

  if (wave < 4) {
    const int w = wave;
    __builtin_amdgcn_s_setprio(2);
    const unsigned loff = (lane >> 3) * 384 + (lane & 7) * 16;
    const unsigned wbase = h * SLAB + (m * 4 + w) * 3072;
    const rsrc_t rk = mk(a.k, KVB), rv = mk(a.v, KVB);
    v4u ring[4][3], pre[NPRE][3];
    auto ld = [&](const rsrc_t& rs, unsigned so, int n) { return __builtin_amdgcn_raw_buffer_load_b128(rs, loff + (n % 3) * 128, so + (n / 3) * ROUND, NT); };
    auto issue = [&](int g, const rsrc_t& r0, unsigned s0, const rsrc_t& r1, unsigned s1) {
#pragma unroll
      for (int n = 3 * g + LMAX; n < 3 * g + LMAX + 3; ++n) ring[(n / 3) % 4][n % 3] = n < 48 ? ld(r0, s0, n) : ld(r1, s1, n - 48);
      SB(); };
    if (w == 0 && lane < 32) fl[lane] = 0u;
    BARRIER(); SB();
#pragma unroll
    for (int n = 0; n < 3 * NPRE + LMAX; ++n) {
      if (n >= LMID) VM_WAIT(LMID - 1);
      if (n < 3 * NPRE) pre[n / 3][n % 3] = ld(rk, wbase, n); else ring[(n / 3) % 4][n % 3] = ld(rk, wbase, n);
      SB(); }
    STAMP(1);
    BARRIER();
    STAMP(2);
    v4u q[3];
    float s[16];
    auto loadq = [&](int b) {
#pragma unroll
      for (int j = 0; j < 3; ++j) q[j] = *(const LAS v4u*)(L + L_Q + b * 384 + (lane & 7) * 16 + j * 128); };
    auto score = [&](const v4u(&r)[3]) -> float {
      float d0 = 0.f, d1 = 0.f, d2 = 0.f;
#pragma unroll
      for (int k = 0; k < 4; ++k) { d0 = dot2(r[0][k], q[0][k], d0); d1 = dot2(r[1][k], q[1][k], d1); d2 = dot2(r[2][k], q[2][k], d2); }
      float d = (d0 + d1) + d2;
      d += dpp<0xB1>(d); d += dpp<0x4E>(d); d += dpp<0x141>(d);
      d = rbf(rbf(d) * 0.07216878364870322f);
      asm volatile("" : "+v"(d));
      return d; };
    int mode = 1;
    auto wait_step = [&] { if (mode == 0) VM_WAIT(LLO - 3); else if (mode == 1) VM_WAIT(LMID - 3); else VM_WAIT(LMAX - 3); };
    unsigned now, tauv, basev;
    auto pace_a = [&](int g) { if ((g & 1) == 0) { now = clock10(); tauv = fl[F_TAU]; basev = fl[F_BASE]; SB(); } };
    auto pace_b = [&](int p, int g) { if ((g & 1) == 0) {
      const int n = p * 16 + g;
      if (w == 0 && lane == 0) { if (g == 0) fl[F_TIME + p] = now; fl[F_STEP] = n; }
      const int tau = __builtin_amdgcn_readfirstlane(tauv), lead = (int)(__builtin_amdgcn_readfirstlane(basev) + ((n * tau) >> 8) - now);
      mode = tau == 0 ? 1 : lead > BLO ? 0 : lead < -BHI ? 2 : 1; } };
    loadq(0);
#pragma unroll
    for (int g = NPRE; g < 16; ++g) {
      VM_WAIT(LMID - 3);
      issue(g, rk, wbase, rv, wbase);
      s[g] = score(ring[g % 4]);
      s[g - NPRE] = score(pre[g - NPRE]);
      SB(); }
    const int b3 = (lane >> 3) & 1, a0 = 12 * (lane >> 5) + 6 * ((lane >> 4) & 1) + 3 * b3;
#pragma unroll 1
    for (int b = 0;; ++b) {
      const unsigned sb = wbase + b * (32 * SLAB);
      const rsrc_t rn = mk(a.k, b < 3 ? KVB : 0u);
      const unsigned nb = b < 3 ? sb + 32 * SLAB : 0u;
      float acc[24];
#pragma unroll
      for (int g = 0; g < 16; ++g) {
        wait_step();
        issue(g, rv, sb, rn, nb);
        pace_a(g);
        if (g == 0) {
          float mx = s[0], l = 0.f;
#pragma unroll
          for (int i = 1; i < 16; ++i) mx = fmaxf(mx, s[i]);
          mx = fmaxf(mx, dpp<0x128>(mx));
          { auto r = __builtin_amdgcn_permlane16_swap(asu(mx), asu(mx), false, false); mx = fmaxf(asf(r[0]), asf(r[1])); }
          { auto r = __builtin_amdgcn_permlane32_swap(asu(mx), asu(mx), false, false); mx = fmaxf(asf(r[0]), asf(r[1])); }
#pragma unroll
          for (int i = 0; i < 16; ++i) { s[i] = exp2_((s[i] - mx) * LOG2E); l += s[i]; }
          l += dpp<0x128>(l); l = sw16(l, l); l = sw32(l, l);
          if (lane == 0) *(LAS v2f*)(L + L_ML + (b * 4 + w) * 8) = v2f{mx, l};
          lds_add(L, F_ML + b, lane);
#pragma unroll
          for (int k = 0; k < 24; ++k) acc[k] = 0.f; }
#pragma unroll
        for (int j = 0; j < 3; ++j)
#pragma unroll
          for (int k = 0; k < 4; ++k) { const unsigned u = ring[g % 4][j][k];
            acc[j * 8 + 2 * k] = fmaf(s[g], lo16(u), acc[j * 8 + 2 * k]);
            acc[j * 8 + 2 * k + 1] = fmaf(s[g], hi16(u), acc[j * 8 + 2 * k + 1]); }
#pragma unroll
        for (int k = 0; k < 24; ++k) asm volatile("" : "+v"(acc[k]));
        pace_b(2 * b + 1, g);
        if (g >= 12 && b == 3) mode = 2;
        SB(); }
#pragma unroll
      for (int i = 0; i < 12; ++i) acc[i] = sw32(acc[i], acc[i + 12]);
#pragma unroll
      for (int i = 0; i < 6; ++i) acc[i] = sw16(acc[i], acc[i + 6]);
      LAS float* part = (LAS float*)(L + L_PART + (b * 4 + w) * 768);
#pragma unroll
      for (int t = 0; t < 3; ++t) { const float u0 = acc[t] + dpp<0x128>(acc[t]), u1 = acc[t + 3] + dpp<0x128>(acc[t + 3]);
        const int ai = a0 + t; part[64 * (ai >> 3) + 8 * (lane & 7) + (ai & 7)] = b3 ? u1 : u0; }
      lds_add(L, F_PART + b, lane);
      if (b == 3) break;
      lds_wait_ge(L, F_Q + b + 1, 1u);
      loadq(b + 1);
#pragma unroll
      for (int g = 0; g < 16; ++g) {
        wait_step();
        issue(g, rk, nb, rv, nb);
        pace_a(g);
        s[g] = score(ring[g % 4]);
        pace_b(2 * b + 2, g);
        SB(); }
    }
    { const rsrc_t rw = mk(a.ws, WS_END);
      unsigned z = __builtin_amdgcn_raw_buffer_load_b32(rw, lane < 5 ? lane * 64 : MASKED, WS_YP + (w * 32 + hs) * 2560 + m * 320, SC1);
      if (w == 1) z |= __builtin_amdgcn_raw_buffer_load_b32(rw, (wg & 63) < NRED && lane < 10 ? lane * 64 : MASKED, WS_SP + ((wg >> 6) * NREP + (wg & (NREP - 1))) * 640, SC1);
      asm volatile("" :: "v"(z)); }
  } else {
    const int j = wave - 4, c = lane & 15, qd = lane >> 4;
    const rsrc_t rws = mk(a.ws, WS_END), rx = mk(a.x, 5120);
    LAS unsigned char* stg = L + L_STG + j * 7680;
    unsigned long long e0 = *(const __attribute__((address_space(4))) unsigned long long*)a.ws;
    v4u xr[5], wr[8]; v2u xo[5];
#pragma unroll
    for (int i = 0; i < 5; ++i) xr[i] = __builtin_amdgcn_raw_buffer_load_b128(rx, qd * 1280 + c * 16 + i * 256, 0, 0);
#pragma unroll
    for (int i = 0; i < 5; ++i) xo[i] = __builtin_amdgcn_raw_buffer_load_b64(rx, (c >> 2) * 1280 + qd * 8 + i * 32, ((m >> 2) * 4 + j) * 160, 0);
    SB();
    { const rsrc_t rq = mk(a.wq, 640 * 12288);
      const int off = lane < 60 ? (lane / 6) * 12288 + (lane % 6) * 16 : MASKED;
#pragma unroll
      for (int i = 0; i < 8; ++i) wr[i] = __builtin_amdgcn_raw_buffer_load_b128(rq, off, (((m >> 2) * 4 + j) * 80 + i * 10) * 12288 + hs * 384 + (m & 3) * 96, NT); }
    SB();
    __builtin_amdgcn_s_barrier();
    SB(); FENCE();
    STAMP(8);
    float sum = 0.f;
#pragma unroll
    for (int i = 0; i < 5; ++i)
#pragma unroll
      for (int k = 0; k < 4; ++k) sum += lo16(xr[i][k]) + hi16(xr[i][k]);
    const float mean0 = row_sum(sum) * (1.f / 640.f);
    float sq = 0.f;
#pragma unroll
    for (int i = 0; i < 5; ++i)
#pragma unroll
      for (int k = 0; k < 4; ++k) { const float d0 = lo16(xr[i][k]) - mean0, d1 = hi16(xr[i][k]) - mean0; sq = fmaf(d0, d0, fmaf(d1, d1, sq)); }
    float mm, rinv; ln_consts(mean0, row_sum(sq) * (1.f / 640.f), mm, rinv);
    { const int src = (c >> 2) * 64; mm = asf(__builtin_amdgcn_ds_bpermute(src, asu(mm))); rinv = asf(__builtin_amdgcn_ds_bpermute(src, asu(rinv))); }
    v4s xb[5];
#pragma unroll
    for (int i = 0; i < 5; ++i) {
      xb[i] = __builtin_bit_cast(v4s, v2u{pk(rbf(lo16(xo[i].x) - mm) * rinv, rbf(hi16(xo[i].x) - mm) * rinv), pk(rbf(lo16(xo[i].y) - mm) * rinv, rbf(hi16(xo[i].y) - mm) * rinv)});
      asm volatile("" : "+v"(xb[i])); }
    asm volatile("" : "+s"(e0));
    SB();
    const Tag T = make_tag(e0);
    STAMP(10);
    if (lane < 60) {
#pragma unroll
      for (int i = 0; i < 8; ++i) *(LAS v4u*)(stg + i * 960 + lane * 16) = wr[i]; }
    { const int troff = (4 * qd + (c >> 2)) * 96 + (c & 3) * 8;
      v4f d[3];
#pragma unroll
      for (int t = 0; t < 3; ++t) {
        d[t] = v4f{0.f, 0.f, 0.f, 0.f};
#pragma unroll
        for (int i = 0; i < 5; ++i)
          d[t] = __builtin_amdgcn_mfma_f32_16x16x16bf16_1k(xb[i], __builtin_amdgcn_ds_read_tr16_b64_v4i16((LAS v4s*)(stg + troff + i * 1536 + t * 32)), d[t], 0, 0, 0); }
      LAS v4f* qp = (LAS v4f*)(L + L_PART);
      qp[j * 64 + lane] = v4f{d[0].x, d[1].x, d[2].x, 0.f};
      lds_add(L, F_QS, lane);
      if (j == 0) {
        lds_wait_ge(L, F_QS, 4u);
        const v4f t = (qp[lane] + qp[64 + lane]) + (qp[128 + lane] + qp[192 + lane]);
        __builtin_amdgcn_raw_buffer_store_b128(v4u{asu(t.x), asu(t.y), asu(t.z), T.lo}, rws, lane * 16, WS_QP + (hs * 8 + m) * 1024, SC1); } }
    STAMP(1);
    if (j > 0) { BARRIER(); __builtin_amdgcn_s_sleep(8); } else __builtin_amdgcn_s_sleep(40);
    { v4u g0, g1;
#pragma unroll 1
      for (int i = 0; i < SPIN; ++i) {
        FENCE();
        g0 = __builtin_amdgcn_raw_buffer_load_b128(rws, qd * 1024 + c * 16, WS_QP + h * 8192 + j * 256, SC1);
        g1 = __builtin_amdgcn_raw_buffer_load_b128(rws, qd * 1024 + c * 16, WS_QP + h * 8192 + 4096 + j * 256, SC1);
        if (__all(g0.w == T.lo && g1.w == T.lo)) break;
        __builtin_amdgcn_s_sleep(8); }
      LAS __bf16* qs = (LAS __bf16*)(L + L_Q + j * 384) + 48 * qd + c;
      qs[0] = (__bf16)(asf(g0.x) + asf(g1.x)); qs[16] = (__bf16)(asf(g0.y) + asf(g1.y)); qs[32] = (__bf16)(asf(g0.z) + asf(g1.z)); }
    STAMP(2);
    if (j == 0) BARRIER(); else lds_add(L, F_Q + j, lane);
    const float b2 = lo16(*(const unsigned short*)a.b2);
    if (j < 3) {
      { const rsrc_t r1 = mk(a.w1, 3072), r2 = mk(a.b1, 3072), r3 = mk(a.w2, 3072);
        unsigned mr[12];
#pragma unroll
        for (int i = 0; i < 4; ++i) { mr[i] = __builtin_amdgcn_raw_buffer_load_b32(r1, lane * 4 + i * 256, j * 1024, 0);
          mr[4 + i] = __builtin_amdgcn_raw_buffer_load_b32(r2, lane * 4 + i * 256, j * 1024, 0);
          mr[8 + i] = __builtin_amdgcn_raw_buffer_load_b32(r3, lane * 4 + i * 256, j * 1024, 0); }
#pragma unroll
        for (int i = 0; i < 12; ++i) *(LAS v2f*)(L + L_MLP + (i / 4) * 6144 + j * 2048 + (i % 4) * 512 + lane * 8) = v2f{lo16(mr[i]), hi16(mr[i])}; }
      const unsigned e = wg * 28 + min(c, 13) * 2, neg = e >= TB_N, bits = (e - neg * TB_N + TB_LO) | (neg << 15);
      const v2f t2 = {lo16(bits), lo16(bits + 1u)};
      v2f ac = {0.f, 0.f};
      const LAS v4f* mw = (const LAS v4f*)(L + L_MLP + j * 2048 + qd * 512);
#pragma unroll 2
      for (int i = 0; i < 32; ++i) {
        const v4f w1 = mw[i], b1 = mw[384 + i], w2 = mw[768 + i];
#pragma unroll
        for (int k = 0; k < 4; ++k) ac += gelu2(t2 * w1[k] + b1[k]) * w2[k]; }
      ac.x = sw16(ac.x, ac.x); ac.y = sw16(ac.y, ac.y); ac.x = sw32(ac.x, ac.x); ac.y = sw32(ac.y, ac.y);
      LAS v2f* red = (LAS v2f*)(L + L_RED);
      if (lane < 16) red[j * 16 + lane] = ac;
      lds_add(L, F_TB, lane);
      if (j == 0) {
        lds_wait_ge<1>(L, F_TB, 3u);
        const v2f y = (red[c] + red[16 + c]) + red[32 + c];
        LAS unsigned short* tg = (LAS unsigned short*)(L + L_RED + 384);
        const unsigned yb = pk(rbf(y.x) + b2, rbf(y.y) + b2);
        if (lane < 14) { tg[(2 * c) / 7 * 8 + (2 * c) % 7] = (unsigned short)yb; tg[(2 * c + 1) / 7 * 8 + (2 * c + 1) % 7] = (unsigned short)(yb >> 16); }
        if (lane < 4) tg[lane * 8 + 7] = (unsigned short)T.lo;
        __builtin_amdgcn_raw_buffer_store_b128(*(const LAS v4u*)(tg + (lane & 3) * 8), rws, lane < 4 ? lane * 16 : MASKED, WS_TB + wg * 64, SC1); }
      { const rsrc_t ro = mk(a.wo, 6144 * 1280);
        const int off = lane < 60 ? (lane / 10) * 1280 + (lane % 10) * 16 : MASKED;
#pragma unroll 1
        for (int p = j; p < 32; p += 3) {
          lds_wait_ge<8>(L, F_STEP, 24u + 2u * p);
          const v4u t = __builtin_amdgcn_raw_buffer_load_b128(ro, off, (hs * 192 + p * 6) * 1280 + m * 160, NT);
          if (lane < 60) *(LAS v4u*)(L + L_STG + p * 960 + lane * 16) = t; }
        lds_add(L, F_WO, lane); }
      if (j == 2 && (wg & 63) < NRED) {
        unsigned bad = 0;
#pragma unroll 1
        for (int p = 0; p < 16; ++p) {
          lds_wait_ge<8>(L, F_STEP, 88u + p);
          const v4u t = __builtin_amdgcn_raw_buffer_load_b128(rws, lane * 16, WS_TB + p * 1024, SC1);
          bad |= (t.w >> 16) ^ (T.lo & 0xffffu);
          *(LAS v4u*)(L + L_TB + p * 1024 + lane * 16) = t; }
        LDS_WAIT();
        if (__all(bad == 0u) && lane == 0) fl[F_HOT] = 1u; }
    } else {
      int prev = 0;
#pragma unroll 1
      for (int k = 0; k < 3; ++k) {
        const int n0 = 32 * (k + 1);
        lds_wait_ge<2>(L, F_STEP, n0);
        const unsigned own = __builtin_amdgcn_readfirstlane(fl[F_TIME + 2 * k + 2]);
        __builtin_amdgcn_raw_buffer_store_b64(v2u{own, T.lo}, rws, lane == 0 ? (wg >> 3) * CSTR + (wg & 7) * 8 : MASKED, WS_CP + k * 32 * CSTR, SC1);
        int sum = 0, cnt = 0;
#pragma unroll 1
        for (int tries = 0; tries < 2 && cnt < 32; ++tries) {
#pragma unroll 1
          for (int it = 0; it < SPIN; ++it) { if ((int)(clock10() - own) >= DLY + 60 * tries) break; __builtin_amdgcn_s_sleep(4); }
          sum = 0; cnt = 0;
#pragma unroll 1
          for (int q = 0; q < 4; q += 2) { const char* pb = (const char*)a.ws + WS_CP + k * 32 * CSTR;
            const char *p0 = pb + (((wg >> 3) + q * 8) & 31) * CSTR, *p1 = pb + (((wg >> 3) + q * 8 + 8) & 31) * CSTR; v16u e, f;
            asm volatile("s_load_dwordx16 %0, %2, 0x0 glc\n\ts_load_dwordx16 %1, %3, 0x0 glc\n\ts_waitcnt lgkmcnt(0)" : "=&s"(e), "=&s"(f) : "s"(p0), "s"(p1) : "memory");
#pragma unroll
            for (int u = 0; u < 8; ++u) { const bool ok = e[2 * u + 1] == T.lo, ol = f[2 * u + 1] == T.lo;
              sum += (ok ? (int)(e[2 * u] - own) : 0) + (ol ? (int)(f[2 * u] - own) : 0); cnt += ok + ol; } } }
        const int lag = cnt ? (int)((float)sum * __builtin_amdgcn_rcpf((float)cnt)) : 0, mean = (int)own + lag;
        if (k > 0) { const int tau = (((mean - prev) * TAUB) >> 5) + (int)((float)(lag * 256) * __builtin_amdgcn_rcpf((float)(CONV - n0)));
          if (lane == 0) { fl[F_BASE] = (int)own - ((n0 * tau) >> 8); fl[F_TAU] = tau; } }
        prev = mean; } }
    v8b wf[6][5];
    { lds_wait_ge<4>(L, F_WO, 3u);
      const int tro = L_STG + (8 * qd + (c >> 2)) * 160 + (c & 3) * 8;
#pragma unroll
      for (int s = 0; s < 6; ++s)
#pragma unroll
        for (int t = 0; t < 5; ++t) {
          const v4s p0 = __builtin_amdgcn_ds_read_tr16_b64_v4i16((LAS v4s*)(L + tro + s * 5120 + t * 32));
          const v4s p1 = __builtin_amdgcn_ds_read_tr16_b64_v4i16((LAS v4s*)(L + tro + s * 5120 + t * 32 + 640));
          wf[s][t] = __builtin_bit_cast(v8b, __builtin_shufflevector(p0, p1, 0, 1, 2, 3, 4, 5, 6, 7)); }
#pragma unroll
      for (int s = 0; s < 6; ++s) asm volatile("" : "+v"(wf[s][0]), "+v"(wf[s][1]), "+v"(wf[s][2]), "+v"(wf[s][3]), "+v"(wf[s][4])); }
    const int rj = wg - 64 * j, rm = (rj & 63) / 5, rs5 = (rj & 63) - 5 * rm, rcol = 80 * rm + (rs5 < 4 ? 16 * qd + 4 * rs5 + (lane & 3) : 64 + 4 * (lane & 3) + qd);
    const bool reducer = rj >= 0 && rj < NRED;
    const float xv = lo16(__builtin_amdgcn_raw_buffer_load_b16(rx, reducer ? rcol * 2 : MASKED, j * 1280, 0));
    float sc[4], M, ls;
    { lds_wait_ge<4>(L, F_ML + j, 4u);
      const v4f p0 = *(const LAS v4f*)(L + L_ML + j * 32), p1 = *(const LAS v4f*)(L + L_ML + j * 32 + 16);
      M = fmaxf(fmaxf(p0.x, p0.z), fmaxf(p1.x, p1.z));
      sc[0] = exp2_((p0.x - M) * LOG2E); sc[1] = exp2_((p0.z - M) * LOG2E); sc[2] = exp2_((p1.x - M) * LOG2E); sc[3] = exp2_((p1.z - M) * LOG2E);
      ls = fmaf(sc[3], p1.w, fmaf(sc[2], p1.y, fmaf(sc[1], p0.w, sc[0] * p0.y)));
      asm volatile("" : "+v"(sc[0]), "+v"(sc[1]), "+v"(sc[2]), "+v"(sc[3]), "+v"(M), "+v"(ls)); }
    STAMP(3);
    __builtin_amdgcn_s_setprio(3);
    const int goff = lane < 52 ? lane * 16 : MASKED;
    lds_wait_ge(L, F_PART + 3, 4u);
    { const LAS v4f* pp = (const LAS v4f*)(L + L_PART + j * 3072) + min(lane, 47);
      v4f o = ((sc[0] * pp[0] + sc[1] * pp[48]) + sc[2] * pp[96]) + sc[3] * pp[144];
      if (lane >= 48) o = v4f{M, ls, 0.f, 0.f};
      __builtin_amdgcn_raw_buffer_store_b128(pack4(o, T), rws, goff, WS_OP + ((j * 32 + h) * 8 + m) * 1024, SC1); }
    STAMP(4);
    { v4u g[8]; float wk;
#pragma unroll 1
      for (int i = 0; i < SPIN; ++i) {
        FENCE();
        const v4u gs = __builtin_amdgcn_raw_buffer_load_b128(rws, (lane & 7) * 1024 + 768, WS_OP + (j * 32 + hs) * 8192, SC1) ^ T.t;
#pragma unroll
        for (int k = 0; k < 8; ++k) g[k] = __builtin_amdgcn_raw_buffer_load_b128(rws, goff, WS_OP + ((j * 32 + hs) * 8 + k) * 1024, SC1);
        SB();
        const float Mk = asf(gs.x);
        float Mg = fmaxf(Mk, dpp<0xB1>(Mk)); Mg = fmaxf(Mg, dpp<0x4E>(Mg)); Mg = fmaxf(Mg, dpp<0x141>(Mg));
        wk = exp2_((Mk - Mg) * LOG2E);
        float Lg = wk * asf(gs.y);
        Lg += dpp<0xB1>(Lg); Lg += dpp<0x4E>(Lg); Lg += dpp<0x141>(Lg);
        wk *= recip(Lg);
        asm volatile("" : "+v"(wk));
        SB();
        unsigned bad = 0;
#pragma unroll
        for (int k = 0; k < 8; ++k) { g[k] ^= T.t; bad |= or4(g[k]); }
        if (__all((((lane < 48 ? bad : 0u) | or4(gs)) & 1023u) == 0u)) break; }
      STAMP(5);
      v4f o = {0.f, 0.f, 0.f, 0.f};
#pragma unroll
      for (int k = 0; k < 8; ++k) o += rdl(wk, k) * __builtin_bit_cast(v4f, g[k]);
      LAS unsigned char* ob = L + L_OB + j * 384;
      if (lane < 48) *(LAS v2u*)(ob + lane * 8) = v2u{pk(o.x, o.y), pk(o.z, o.w)};
      v4f y[5];
#pragma unroll
      for (int t = 0; t < 5; ++t) y[t] = v4f{0.f, 0.f, 0.f, 0.f};
#pragma unroll
      for (int s = 0; s < 6; ++s) {
        const v8b of = *(const LAS v8b*)(ob + s * 64 + qd * 16);
#pragma unroll
        for (int t = 0; t < 5; ++t) y[t] = __builtin_amdgcn_mfma_f32_16x16x32_bf16(of, wf[s][t], y[t], 0, 0, 0); }
      const v4f y4 = {dpp<0x00>(y[4].x), dpp<0x55>(y[4].x), dpp<0xAA>(y[4].x), dpp<0xFF>(y[4].x)};
      const v4f ys = qd == 0 ? v4f{y[0].x, y[1].x, y[2].x, y[3].x} : y4;
      __builtin_amdgcn_raw_buffer_store_b128(pack4(ys, T), rws, qd == 0 ? c * 16 : (qd == 1 && (c & 3) == 0) ? 256 + c * 4 : MASKED, WS_YP + (j * 32 + hs) * 2560 + m * 320, SC1); }
    STAMP(6);
    if (reducer) {
      const int yoff = (lane >> 2) * 2560 + (lane & 3) * 16;
      v4u g0, g1;
#pragma unroll 1
      for (int i = 0; i < SPIN; ++i) {
        FENCE();
        g0 = __builtin_amdgcn_raw_buffer_load_b128(rws, yoff, WS_YP + j * 32 * 2560 + rj * 64, SC1) ^ T.t;
        g1 = __builtin_amdgcn_raw_buffer_load_b128(rws, yoff, WS_YP + (j * 32 + 16) * 2560 + rj * 64, SC1) ^ T.t;
        if (__all(((or4(g0) | or4(g1)) & 1023u) == 0u)) break; }
      STAMP(7);
      const v4f y4 = __builtin_bit_cast(v4f, g0) + __builtin_bit_cast(v4f, g1);
      float y = sw16(sw32(y4.x, y4.z), sw32(y4.y, y4.w));
      y += dpp<0x124>(y); y += dpp<0x128>(y);
      const float hv = rbf(xv + rbf(y));
      float s1 = hv, s2 = hv * hv;
      s1 += dpp<0xB1>(s1); s2 += dpp<0xB1>(s2); s1 += dpp<0x4E>(s1); s2 += dpp<0x4E>(s2);
      s1 = sw16(s1, s1); s2 = sw16(s2, s2); s1 = sw32(s1, s1); s2 = sw32(s2, s2);
      __builtin_amdgcn_raw_buffer_store_b128(v4u{asu(s1), asu(s2), T.lo, T.hi}, rws, lane < NREP ? lane * 640 : MASKED, WS_SP + j * NREP * 640 + rj * 16, SC1);
      STAMP(9);
      const bool hot = __builtin_amdgcn_readfirstlane(fl[F_HOT]) != 0u;
      v4u sg;
#pragma unroll 1
      for (int i = 0; i < SPIN; ++i) {
        FENCE();
        sg = __builtin_amdgcn_raw_buffer_load_b128(rws, lane < NRED ? lane * 16 : MASKED, WS_SP + (j * NREP + (wg & (NREP - 1))) * 640, SC1);
        if (__all((sg.z == T.lo && sg.w == T.hi) || lane >= NRED)) break; }
      STAMP(11);
      const float mean = wave_sum(asf(sg.x)) * (1.f / 640.f), var = fmaxf(fmaf(wave_sum(asf(sg.y)), 1.f / 640.f, -mean * mean), 0.f);
      float m1, r1, m2, r2;
      ln_consts(mean, var, m1, r1);
      ln_consts((mean - m1) * r1, var * r1 * r1, m2, r2);
      const float tn = rbf(rbf(rbf(rbf(hv - m1) * r1) - m2) * r2);
      const unsigned tb = asu(tn) >> 16, mag = (tb & 0x7fffu) - TB_LO, e = min(mag, TB_N - 1u) + (tb >> 15) * TB_N, e7 = (e * 9363u) >> 16;
      float yv = lo16(*(const LAS unsigned short*)(L + L_TB + (e + e7) * 2));
      const bool own = (lane & 12) == 0;
      unsigned long long miss = __ballot(own && (mag >= TB_N || !hot));
#pragma unroll 1
      for (; miss; miss &= miss - 1ull) {
        const int src = __builtin_ctzll(miss);
        const float t = rdl(tn, src);
        v2f ac = {0.f, 0.f};
#pragma unroll 1
        for (int i = 0; i < 12; ++i) { const LAS v2f* mw = (const LAS v2f*)(L + L_MLP) + i * 64 + lane; ac += gelu2(t * mw[0] + mw[768]) * mw[1536]; }
        const float yd = rbf(rbf(wave_sum(ac.x + ac.y)) + b2);
        yv = lane == src ? yd : yv; }
      STAMP(12);
      if (own) ((__bf16*)a.out)[j * 640 + rcol] = (__bf16)(hv + yv);
      if (wg == 0 && lane == 0) __builtin_amdgcn_raw_buffer_store_b64(v2u{T.lo, T.hi}, rws, 0, 0, SC1); } }
}

extern "C" void kernel_launch(void* const* d_in, const int* in_sizes, int n_in, void* d_out, int out_size, void* d_ws, size_t ws_size, hipStream_t stream) {
  Args a{d_in[0], d_in[1], d_in[2], d_in[3], d_in[4], d_in[6], d_in[7], d_in[8], d_in[9], d_out, d_ws};
  fused_decode<<<256, 512, 0, stream>>>(a);
}
```

```cpp
#include <hip/hip_runtime.h>
#include <hip/hip_bf16.h>


#define LAS __attribute__((address_space(3)))
typedef unsigned v4u __attribute__((ext_vector_type(4)));
typedef unsigned v2u __attribute__((ext_vector_type(2)));
typedef float v4f __attribute__((ext_vector_type(4)));
typedef float v2f __attribute__((ext_vector_type(2)));
typedef short v4s __attribute__((ext_vector_type(4)));
typedef __bf16 v8b __attribute__((ext_vector_type(8)));
typedef __bf16 v2b __attribute__((ext_vector_type(2)));
typedef __amdgpu_buffer_rsrc_t rsrc_t;

namespace {
constexpr int LLO = 5, LMID = 7, LMAX = 9, NPRE = 8;
constexpr int BLO = 20, BHI = 40, TAUB = 248, DLY = 200, CONV = 124;
constexpr int NREP = 8, CSTR = 256, NRED = 40;
constexpr unsigned SLAB = 4096u * 384u, KVB = 128u * SLAB, ROUND = 32u * 3072u;
constexpr unsigned WS_QP = 4096, WS_OP = WS_QP + 256 * 1024, WS_YP = WS_OP + 4 * 256 * 1024, WS_TB = WS_YP + 128 * 2560, WS_SP = WS_TB + 256 * 64,
                   WS_CP = WS_SP + 4 * NREP * 640, WS_END = WS_CP + 3 * 32 * CSTR;
constexpr int L_Q = 0, L_FLAG = 1536, L_ML = 1664, L_OB = 1792, L_RED = L_OB + 4 * 384, L_PART = L_RED + 512, L_MLP = L_PART + 16 * 768, L_STG = L_MLP + 3 * 6144,
              L_TB = L_STG + 4 * 7680, L_END = L_TB + 16384;
constexpr int F_PART = 0, F_Q = 4, F_TB = 8, F_WO = 9, F_STEP = 10, F_HOT = 11, F_TAU = 12, F_BASE = 13, F_TIME = 16, F_ML = 24, F_QS = 28;
constexpr unsigned TB_LO = 102u * 128u, TB_N = 28u * 128u;
constexpr int MASKED = 0x40000000, NT = 2, SC1 = 16;
constexpr int SPIN = 1 << 15;
constexpr float LOG2E = 1.4426950408889634f;

__device__ __forceinline__ rsrc_t mk(const void* p, unsigned n) { return __builtin_amdgcn_make_buffer_rsrc((void*)p, 0, (int)n, 0x00020000); }
__device__ __forceinline__ float asf(unsigned u) { return __builtin_bit_cast(float, u); }
__device__ __forceinline__ unsigned asu(float f) { return __builtin_bit_cast(unsigned, f); }
__device__ __forceinline__ float lo16(unsigned u) { return asf(u << 16); }
__device__ __forceinline__ float hi16(unsigned u) { return asf(u & 0xffff0000u); }
__device__ __forceinline__ unsigned pk(float a, float b) { return __builtin_bit_cast(unsigned, v2b{(__bf16)a, (__bf16)b}); }
__device__ __forceinline__ float rbf(float x) { return (float)(__bf16)x; }
__device__ __forceinline__ float dot2(unsigned a, unsigned b, float c) {
  return __builtin_amdgcn_fdot2_f32_bf16(__builtin_bit_cast(v2b, a), __builtin_bit_cast(v2b, b), c, false); }
template <int C> __device__ __forceinline__ float dpp(float x) { return asf(__builtin_amdgcn_mov_dpp(asu(x), C, 0xf, 0xf, true)); }
__device__ __forceinline__ float sw16(float a, float b) { auto r = __builtin_amdgcn_permlane16_swap(asu(a), asu(b), false, false); return asf(r[0]) + asf(r[1]); }
__device__ __forceinline__ float sw32(float a, float b) { auto r = __builtin_amdgcn_permlane32_swap(asu(a), asu(b), false, false); return asf(r[0]) + asf(r[1]); }
__device__ __forceinline__ float row_sum(float x) { x += dpp<0x128>(x); x += dpp<0x124>(x); x += dpp<0x122>(x); x += dpp<0x121>(x); return x; }
__device__ __forceinline__ float wave_sum(float x) { x = row_sum(x); x = sw16(x, x); return sw32(x, x); }
__device__ __forceinline__ float exp2_(float x) { return __builtin_amdgcn_exp2f(x); }
__device__ __forceinline__ float recip(float x) { const float r = __builtin_amdgcn_rcpf(x); return fmaf(fmaf(-x, r, 1.f), r, r); }
__device__ __forceinline__ float rdl(float x, int l) { return asf(__builtin_amdgcn_readlane(asu(x), l)); }
__device__ __forceinline__ unsigned clock10() { return (unsigned)__builtin_amdgcn_s_memrealtime(); }
__device__ __forceinline__ void ln_consts(float mean, float var, float& m, float& rinv) {
  m = rbf(mean); rinv = recip(rbf(__builtin_amdgcn_sqrtf(rbf(rbf(var) + 1e-5f)))); }
__device__ __forceinline__ v2f gelu2(v2f u) {
  const v2f z = u * (u * u * (-0.044715f * 1.5957691216057308f * LOG2E) + (-1.5957691216057308f * LOG2E));
  const v2f d = v2f{exp2_(z.x), exp2_(z.y)} + 1.f;
  return u * v2f{__builtin_amdgcn_rcpf(d.x), __builtin_amdgcn_rcpf(d.y)}; }

struct Tag { unsigned lo, hi; v4u t; };
__device__ __forceinline__ Tag make_tag(unsigned long long e) {
  const unsigned long long t = e + 1ull;
  return Tag{(unsigned)t, (unsigned)(t >> 32), v4u{(unsigned)t & 1023u, (unsigned)(t >> 10) & 1023u, (unsigned)(t >> 20) & 1023u, (unsigned)(t >> 30) & 1023u}}; }
__device__ __forceinline__ unsigned bfi(unsigned m, unsigned a, unsigned b) { unsigned r; asm("v_bfi_b32 %0, %1, %2, %3" : "=v"(r) : "v"(m), "s"(a), "v"(b)); return r; }
__device__ __forceinline__ v4u pack4(v4f v, const Tag& T) {
  return v4u{bfi(1023u, T.t.x, asu(v.x) + 512u), bfi(1023u, T.t.y, asu(v.y) + 512u), bfi(1023u, T.t.z, asu(v.z) + 512u), bfi(1023u, T.t.w, asu(v.w) + 512u)}; }
__device__ __forceinline__ unsigned or4(v4u x) { return (x.x | x.y) | (x.z | x.w); }

#define FENCE() asm volatile("" ::: "memory")
#define SB() __builtin_amdgcn_sched_barrier(0)
#define LDS_WAIT() asm volatile("s_waitcnt lgkmcnt(0)" ::: "memory")
#define BARRIER() asm volatile("s_waitcnt lgkmcnt(0)\n\ts_barrier" ::: "memory")
#define VM_WAIT(n) do { asm volatile("s_waitcnt vmcnt(%0)" ::"n"(n) : "memory"); SB(); } while (0)

template <int SLEEP = 0> __device__ __forceinline__ void lds_wait_ge(LAS unsigned char* L, int word, unsigned want) {
  volatile LAS unsigned* p = (volatile LAS unsigned*)(L + L_FLAG) + word;
#pragma unroll 1
  for (int i = 0; i < (SPIN << 5); ++i) { if (__builtin_amdgcn_readfirstlane(*p) >= want) break; if (SLEEP) __builtin_amdgcn_s_sleep(SLEEP); }
  FENCE(); }
__device__ __forceinline__ void lds_add(LAS unsigned char* L, int word, int lane) {
  LDS_WAIT();
  if (lane == 0) __hip_atomic_fetch_add((LAS unsigned*)(L + L_FLAG) + word, 1u, __ATOMIC_RELAXED, __HIP_MEMORY_SCOPE_WORKGROUP); }
}

struct Args { const void *x, *k, *v, *wq, *wo, *w1, *b1, *w2, *b2; void *out, *ws; };

__global__ void __launch_bounds__(512, 2) fused_decode(Args a) {
  __shared__ __attribute__((aligned(16))) unsigned char lds_raw[L_END];
  LAS unsigned char* L = (LAS unsigned char*)lds_raw;
  volatile LAS unsigned* fl = (volatile LAS unsigned*)(L + L_FLAG);
  const int wg = blockIdx.x, lane = threadIdx.x & 63, h = wg & 31, m = wg >> 5, hs = (h + 1) & 31;
  const int wave = __builtin_amdgcn_readfirstlane(threadIdx.x >> 6);
  asm volatile("" : "+s"(a.x), "+s"(a.k), "+s"(a.v), "+s"(a.wq), "+s"(a.ws));

  if (wave < 4) {
    const int w = wave;
    __builtin_amdgcn_s_setprio(2);
    const unsigned loff = (lane >> 3) * 384 + (lane & 7) * 16;
    const unsigned wbase = h * SLAB + (m * 4 + w) * 3072;
    const rsrc_t rk = mk(a.k, KVB), rv = mk(a.v, KVB);
    v4u ring[4][3], pre[NPRE][3];
    auto ld = [&](const rsrc_t& rs, unsigned so, int n) { return __builtin_amdgcn_raw_buffer_load_b128(rs, loff + (n % 3) * 128, so + (n / 3) * ROUND, NT); };
    auto issue = [&](int g, const rsrc_t& r0, unsigned s0, const rsrc_t& r1, unsigned s1) {
#pragma unroll
      for (int n = 3 * g + LMAX; n < 3 * g + LMAX + 3; ++n) ring[(n / 3) % 4][n % 3] = n < 48 ? ld(r0, s0, n) : ld(r1, s1, n - 48);
      SB(); };
    if (w == 0 && lane < 32) fl[lane] = 0u;
    BARRIER(); SB();
#pragma unroll
    for (int n = 0; n < 3 * NPRE + LMAX; ++n) {
      if (n >= LMID) VM_WAIT(LMID - 1);
      if (n < 3 * NPRE) pre[n / 3][n % 3] = ld(rk, wbase, n); else ring[(n / 3) % 4][n % 3] = ld(rk, wbase, n);
      SB(); }
    BARRIER();
    v4u q[3];
    float s[16];
    auto loadq = [&](int b) {
#pragma unroll
      for (int j = 0; j < 3; ++j) q[j] = *(const LAS v4u*)(L + L_Q + b * 384 + (lane & 7) * 16 + j * 128); };
    auto score = [&](const v4u(&r)[3]) -> float {
      float d0 = 0.f, d1 = 0.f, d2 = 0.f;
#pragma unroll
      for (int k = 0; k < 4; ++k) { d0 = dot2(r[0][k], q[0][k], d0); d1 = dot2(r[1][k], q[1][k], d1); d2 = dot2(r[2][k], q[2][k], d2); }
      float d = (d0 + d1) + d2;
      d += dpp<0xB1>(d); d += dpp<0x4E>(d); d += dpp<0x141>(d);
      d = rbf(rbf(d) * 0.07216878364870322f);
      asm volatile("" : "+v"(d));
      return d; };
    int mode = 1;
    auto wait_step = [&] { if (mode == 0) VM_WAIT(LLO - 3); else if (mode == 1) VM_WAIT(LMID - 3); else VM_WAIT(LMAX - 3); };
    unsigned now, tauv, basev;
    auto pace_a = [&](int g) { if ((g & 1) == 0) { now = clock10(); tauv = fl[F_TAU]; basev = fl[F_BASE]; SB(); } };
    auto pace_b = [&](int p, int g) { if ((g & 1) == 0) {
      const int n = p * 16 + g;
      if (w == 0 && lane == 0) { if (g == 0) fl[F_TIME + p] = now; fl[F_STEP] = n; }
      const int tau = __builtin_amdgcn_readfirstlane(tauv), lead = (int)((unsigned)__builtin_amdgcn_readfirstlane(basev) + (unsigned)((n * tau) >> 8) - now);
      mode = tau == 0 ? 1 : lead > BLO ? 0 : lead < -BHI ? 2 : 1; } };
    loadq(0);
#pragma unroll
    for (int g = NPRE; g < 16; ++g) {
      VM_WAIT(LMID - 3);
      issue(g, rk, wbase, rv, wbase);
      s[g] = score(ring[g % 4]);
      s[g - NPRE] = score(pre[g - NPRE]);
      SB(); }
    const int b3 = (lane >> 3) & 1, a0 = 12 * (lane >> 5) + 6 * ((lane >> 4) & 1) + 3 * b3;
#pragma unroll 1
    for (int b = 0;; ++b) {
      const unsigned sb = wbase + b * (32 * SLAB);
      const rsrc_t rn = mk(a.k, b < 3 ? KVB : 0u);
      const unsigned nb = b < 3 ? sb + 32 * SLAB : 0u;
      v2f ac2[12];
#pragma unroll
      for (int g = 0; g < 16; ++g) {
        wait_step();
        issue(g, rv, sb, rn, nb);
        pace_a(g);
        if (g == 0) {
          float mx = s[0], l = 0.f;
#pragma unroll
          for (int i = 1; i < 16; ++i) mx = fmaxf(mx, s[i]);
          mx = fmaxf(mx, dpp<0x128>(mx));
          { auto r = __builtin_amdgcn_permlane16_swap(asu(mx), asu(mx), false, false); mx = fmaxf(asf(r[0]), asf(r[1])); }
          { auto r = __builtin_amdgcn_permlane32_swap(asu(mx), asu(mx), false, false); mx = fmaxf(asf(r[0]), asf(r[1])); }
#pragma unroll
          for (int i = 0; i < 16; ++i) { s[i] = exp2_((s[i] - mx) * LOG2E); l += s[i]; }
          l += dpp<0x128>(l); l = sw16(l, l); l = sw32(l, l);
          if (lane == 0) *(LAS v2f*)(L + L_ML + (b * 4 + w) * 8) = v2f{mx, l};
          lds_add(L, F_ML + b, lane);
#pragma unroll
          for (int k = 0; k < 12; ++k) ac2[k] = v2f{0.f, 0.f}; }
#pragma unroll
        for (int k = 0; k < 12; ++k) { const unsigned u = ring[g % 4][k / 4][k % 4];
          ac2[k] += s[g] * v2f{lo16(u), hi16(u)};
          asm volatile("" : "+v"(ac2[k])); }
        pace_b(2 * b + 1, g);
        if (g >= 12 && b == 3) mode = 2;
        SB(); }
      float acc[12];
#pragma unroll
      for (int i = 0; i < 12; ++i) acc[i] = sw32(ac2[i / 2][i % 2], ac2[6 + i / 2][i % 2]);
#pragma unroll
      for (int i = 0; i < 6; ++i) acc[i] = sw16(acc[i], acc[i + 6]);
      LAS float* part = (LAS float*)(L + L_PART + (b * 4 + w) * 768);
#pragma unroll
      for (int t = 0; t < 3; ++t) { const float u0 = acc[t] + dpp<0x128>(acc[t]), u1 = acc[t + 3] + dpp<0x128>(acc[t + 3]);
        const int ai = a0 + t; part[64 * (ai >> 3) + 8 * (lane & 7) + (ai & 7)] = b3 ? u1 : u0; }
      lds_add(L, F_PART + b, lane);
      if (b == 3) break;
      lds_wait_ge(L, F_Q + b + 1, 1u);
      loadq(b + 1);
#pragma unroll
      for (int g = 0; g < 16; ++g) {
        wait_step();
        issue(g, rk, nb, rv, nb);
        pace_a(g);
        s[g] = score(ring[g % 4]);
        pace_b(2 * b + 2, g);
        SB(); }
    }
    { const rsrc_t rw = mk(a.ws, WS_END);
      unsigned z = __builtin_amdgcn_raw_buffer_load_b32(rw, lane < 5 ? lane * 64 : MASKED, WS_YP + (w * 32 + hs) * 2560 + m * 320, SC1);
      if (w == 1) z |= __builtin_amdgcn_raw_buffer_load_b32(rw, (wg & 63) < NRED && lane < 10 ? lane * 64 : MASKED, WS_SP + ((wg >> 6) * NREP + (wg & (NREP - 1))) * 640, SC1);
      asm volatile("" :: "v"(z)); }
  } else {
    const int j = wave - 4, c = lane & 15, qd = lane >> 4;
    const rsrc_t rws = mk(a.ws, WS_END), rx = mk(a.x, 5120);
    LAS unsigned char* stg = L + L_STG + j * 7680;
    unsigned long long e0 = *(const __attribute__((address_space(4))) unsigned long long*)a.ws;
    v4u xr[5], wr[8]; v2u xo[5];
#pragma unroll
    for (int i = 0; i < 5; ++i) xr[i] = __builtin_amdgcn_raw_buffer_load_b128(rx, qd * 1280 + c * 16 + i * 256, 0, 0);
#pragma unroll
    for (int i = 0; i < 5; ++i) xo[i] = __builtin_amdgcn_raw_buffer_load_b64(rx, (c >> 2) * 1280 + qd * 8 + i * 32, ((m >> 2) * 4 + j) * 160, 0);
    SB();
    { const rsrc_t rq = mk(a.wq, 640 * 12288);
      const int off = lane < 60 ? (lane / 6) * 12288 + (lane % 6) * 16 : MASKED;
#pragma unroll
      for (int i = 0; i < 8; ++i) wr[i] = __builtin_amdgcn_raw_buffer_load_b128(rq, off, (((m >> 2) * 4 + j) * 80 + i * 10) * 12288 + hs * 384 + (m & 3) * 96, NT); }
    SB();
    __builtin_amdgcn_s_barrier();
    SB(); FENCE();
    float sum = 0.f;
#pragma unroll
    for (int i = 0; i < 5; ++i)
#pragma unroll
      for (int k = 0; k < 4; ++k) sum += lo16(xr[i][k]) + hi16(xr[i][k]);
    const float mean0 = row_sum(sum) * (1.f / 640.f);
    float sq = 0.f;
#pragma unroll
    for (int i = 0; i < 5; ++i)
#pragma unroll
      for (int k = 0; k < 4; ++k) { const float d0 = lo16(xr[i][k]) - mean0, d1 = hi16(xr[i][k]) - mean0; sq = fmaf(d0, d0, fmaf(d1, d1, sq)); }
    float mm, rinv; ln_consts(mean0, row_sum(sq) * (1.f / 640.f), mm, rinv);
    { const int src = (c >> 2) * 64; mm = asf(__builtin_amdgcn_ds_bpermute(src, asu(mm))); rinv = asf(__builtin_amdgcn_ds_bpermute(src, asu(rinv))); }
    v4s xb[5];
#pragma unroll
    for (int i = 0; i < 5; ++i) {
      xb[i] = __builtin_bit_cast(v4s, v2u{pk(rbf(lo16(xo[i].x) - mm) * rinv, rbf(hi16(xo[i].x) - mm) * rinv), pk(rbf(lo16(xo[i].y) - mm) * rinv, rbf(hi16(xo[i].y) - mm) * rinv)});
      asm volatile("" : "+v"(xb[i])); }
    asm volatile("" : "+s"(e0));
    SB();
    const Tag T = make_tag(e0);
    if (lane < 60) {
#pragma unroll
      for (int i = 0; i < 8; ++i) *(LAS v4u*)(stg + i * 960 + lane * 16) = wr[i]; }
    { const int troff = (4 * qd + (c >> 2)) * 96 + (c & 3) * 8;
      v4f d[3];
#pragma unroll
      for (int t = 0; t < 3; ++t) {
        d[t] = v4f{0.f, 0.f, 0.f, 0.f};
#pragma unroll
        for (int i = 0; i < 5; ++i)
          d[t] = __builtin_amdgcn_mfma_f32_16x16x16bf16_1k(xb[i], __builtin_amdgcn_ds_read_tr16_b64_v4i16((LAS v4s*)(stg + troff + i * 1536 + t * 32)), d[t], 0, 0, 0); }
      LAS v4f* qp = (LAS v4f*)(L + L_PART);
      qp[j * 64 + lane] = v4f{d[0].x, d[1].x, d[2].x, 0.f};
      lds_add(L, F_QS, lane);
      if (j == 0) {
        lds_wait_ge(L, F_QS, 4u);
        const v4f t = (qp[lane] + qp[64 + lane]) + (qp[128 + lane] + qp[192 + lane]);
        __builtin_amdgcn_raw_buffer_store_b128(v4u{asu(t.x), asu(t.y), asu(t.z), T.lo}, rws, lane * 16, WS_QP + (hs * 8 + m) * 1024, SC1); } }
    if (j > 0) { BARRIER(); __builtin_amdgcn_s_sleep(8); } else __builtin_amdgcn_s_sleep(40);
    { v4u g0, g1;
#pragma unroll 1
      for (int i = 0; i < SPIN; ++i) {
        FENCE();
        g0 = __builtin_amdgcn_raw_buffer_load_b128(rws, qd * 1024 + c * 16, WS_QP + h * 8192 + j * 256, SC1);
        g1 = __builtin_amdgcn_raw_buffer_load_b128(rws, qd * 1024 + c * 16, WS_QP + h * 8192 + 4096 + j * 256, SC1);
        if (__all(g0.w == T.lo && g1.w == T.lo)) break;
        __builtin_amdgcn_s_sleep(8); }
      LAS __bf16* qs = (LAS __bf16*)(L + L_Q + j * 384) + 48 * qd + c;
      qs[0] = (__bf16)(asf(g0.x) + asf(g1.x)); qs[16] = (__bf16)(asf(g0.y) + asf(g1.y)); qs[32] = (__bf16)(asf(g0.z) + asf(g1.z)); }
    if (j == 0) BARRIER(); else lds_add(L, F_Q + j, lane);
    const float b2 = lo16(*(const unsigned short*)a.b2);
    if (j < 3) {
      { const rsrc_t r1 = mk(a.w1, 3072), r2 = mk(a.b1, 3072), r3 = mk(a.w2, 3072);
        unsigned mr[12];
#pragma unroll
        for (int i = 0; i < 4; ++i) { mr[i] = __builtin_amdgcn_raw_buffer_load_b32(r1, lane * 4 + i * 256, j * 1024, 0);
          mr[4 + i] = __builtin_amdgcn_raw_buffer_load_b32(r2, lane * 4 + i * 256, j * 1024, 0);
          mr[8 + i] = __builtin_amdgcn_raw_buffer_load_b32(r3, lane * 4 + i * 256, j * 1024, 0); }
#pragma unroll
        for (int i = 0; i < 12; ++i) *(LAS v2f*)(L + L_MLP + (i / 4) * 6144 + j * 2048 + (i % 4) * 512 + lane * 8) = v2f{lo16(mr[i]), hi16(mr[i])}; }
      const unsigned e = wg * 28 + min(c, 13) * 2, neg = e >= TB_N, bits = (e - neg * TB_N + TB_LO) | (neg << 15);
      const v2f t2 = {lo16(bits), lo16(bits + 1u)};
      v2f ac = {0.f, 0.f};
      const LAS v4f* mw = (const LAS v4f*)(L + L_MLP + j * 2048 + qd * 512);
#pragma unroll 2
      for (int i = 0; i < 32; ++i) {
        const v4f w1 = mw[i], b1 = mw[384 + i], w2 = mw[768 + i];
#pragma unroll
        for (int k = 0; k < 4; ++k) ac += gelu2(t2 * w1[k] + b1[k]) * w2[k]; }
      ac.x = sw16(ac.x, ac.x); ac.y = sw16(ac.y, ac.y); ac.x = sw32(ac.x, ac.x); ac.y = sw32(ac.y, ac.y);
      LAS v2f* red = (LAS v2f*)(L + L_RED);
      if (lane < 16) red[j * 16 + lane] = ac;
      lds_add(L, F_TB, lane);
      if (j == 0) {
        lds_wait_ge<1>(L, F_TB, 3u);
        const v2f y = (red[c] + red[16 + c]) + red[32 + c];
        LAS unsigned short* tg = (LAS unsigned short*)(L + L_RED + 384);
        const unsigned yb = pk(rbf(y.x) + b2, rbf(y.y) + b2);
        if (lane < 14) { tg[(2 * c) / 7 * 8 + (2 * c) % 7] = (unsigned short)yb; tg[(2 * c + 1) / 7 * 8 + (2 * c + 1) % 7] = (unsigned short)(yb >> 16); }
        if (lane < 4) tg[lane * 8 + 7] = (unsigned short)T.lo;
        __builtin_amdgcn_raw_buffer_store_b128(*(const LAS v4u*)(tg + (lane & 3) * 8), rws, lane < 4 ? lane * 16 : MASKED, WS_TB + wg * 64, SC1); }
      { const rsrc_t ro = mk(a.wo, 6144 * 1280);
        const int off = lane < 60 ? (lane / 10) * 1280 + (lane % 10) * 16 : MASKED;
#pragma unroll 1
        for (int p = j; p < 32; p += 3) {
          lds_wait_ge<8>(L, F_STEP, 24u + 2u * p);
          const v4u t = __builtin_amdgcn_raw_buffer_load_b128(ro, off, (hs * 192 + p * 6) * 1280 + m * 160, NT);
          if (lane < 60) *(LAS v4u*)(L + L_STG + p * 960 + lane * 16) = t; }
        lds_add(L, F_WO, lane); }
      if (j == 2 && (wg & 63) < NRED) {
        unsigned bad = 0;
#pragma unroll 1
        for (int p = 0; p < 16; ++p) {
          lds_wait_ge<8>(L, F_STEP, 88u + p);
          const v4u t = __builtin_amdgcn_raw_buffer_load_b128(rws, lane * 16, WS_TB + p * 1024, SC1);
          bad |= (t.w >> 16) ^ (T.lo & 0xffffu);
          *(LAS v4u*)(L + L_TB + p * 1024 + lane * 16) = t; }
        LDS_WAIT();
        if (__all(bad == 0u) && lane == 0) fl[F_HOT] = 1u; }
    } else {
      unsigned prev = 0;
#pragma unroll 1
      for (int k = 0; k < 3; ++k) {
        const int n0 = 32 * (k + 1);
        lds_wait_ge<2>(L, F_STEP, n0);
        const unsigned own = __builtin_amdgcn_readfirstlane(fl[F_TIME + 2 * k + 2]);
        __builtin_amdgcn_raw_buffer_store_b64(v2u{own, T.lo}, rws, lane == 0 ? (wg >> 3) * CSTR + (wg & 7) * 8 : MASKED, WS_CP + k * 32 * CSTR, SC1);
        float sum = 0.f; int cnt = 0;
#pragma unroll 1
        for (int tries = 0; tries < 2 && cnt < 32; ++tries) {
#pragma unroll 1
          for (int it = 0; it < SPIN; ++it) { if ((int)(clock10() - own) >= DLY + 60 * tries) break; __builtin_amdgcn_s_sleep(4); }
          FENCE();
          const v2u e = __builtin_amdgcn_raw_buffer_load_b64(rws, lane < 32 ? (((wg >> 3) + (lane & 24)) & 31) * CSTR + (lane & 7) * 8 : MASKED, WS_CP + k * 32 * CSTR, SC1);
          const bool ok = lane < 32 && e.y == T.lo;
          cnt = __builtin_popcountll(__ballot(ok));
          sum = wave_sum(ok ? (float)(int)(e.x - own) : 0.f); }
        const float lag = cnt ? sum * __builtin_amdgcn_rcpf((float)cnt) : 0.f;
        const unsigned mean = own + (unsigned)__builtin_amdgcn_readfirstlane((int)lag);
        if (k > 0) { const int tau = (((int)(mean - prev) * TAUB) >> 5) + (int)(lag * 256.f * __builtin_amdgcn_rcpf((float)(CONV - n0)));
          if (lane == 0) { fl[F_BASE] = own - (unsigned)((n0 * tau) >> 8); fl[F_TAU] = tau; } }
        prev = mean; } }
    v8b wf[6][5];
    { lds_wait_ge<4>(L, F_WO, 3u);
      const int tro = L_STG + (8 * qd + (c >> 2)) * 160 + (c & 3) * 8;
#pragma unroll
      for (int s = 0; s < 6; ++s)
#pragma unroll
        for (int t = 0; t < 5; ++t) {
          const v4s p0 = __builtin_amdgcn_ds_read_tr16_b64_v4i16((LAS v4s*)(L + tro + s * 5120 + t * 32));
          const v4s p1 = __builtin_amdgcn_ds_read_tr16_b64_v4i16((LAS v4s*)(L + tro + s * 5120 + t * 32 + 640));
          wf[s][t] = __builtin_bit_cast(v8b, __builtin_shufflevector(p0, p1, 0, 1, 2, 3, 4, 5, 6, 7)); }
#pragma unroll
      for (int s = 0; s < 6; ++s) asm volatile("" : "+v"(wf[s][0]), "+v"(wf[s][1]), "+v"(wf[s][2]), "+v"(wf[s][3]), "+v"(wf[s][4])); }
    const int rj = wg - 64 * j, rm = (rj & 63) / 5, rs5 = (rj & 63) - 5 * rm, rcol = 80 * rm + (rs5 < 4 ? 16 * qd + 4 * rs5 + (lane & 3) : 64 + 4 * (lane & 3) + qd);
    const bool reducer = rj >= 0 && rj < NRED;
    const float xv = lo16(__builtin_amdgcn_raw_buffer_load_b16(rx, reducer ? rcol * 2 : MASKED, j * 1280, 0));
    float sc[4], M, ls; v4u sgr;
    { lds_wait_ge<4>(L, F_ML + j, 4u);
      const v4f p0 = *(const LAS v4f*)(L + L_ML + j * 32), p1 = *(const LAS v4f*)(L + L_ML + j * 32 + 16);
      M = fmaxf(fmaxf(p0.x, p0.z), fmaxf(p1.x, p1.z));
      sc[0] = exp2_((p0.x - M) * LOG2E); sc[1] = exp2_((p0.z - M) * LOG2E); sc[2] = exp2_((p1.x - M) * LOG2E); sc[3] = exp2_((p1.z - M) * LOG2E);
      ls = fmaf(sc[3], p1.w, fmaf(sc[2], p1.y, fmaf(sc[1], p0.w, sc[0] * p0.y)));
      sgr = pack4(v4f{M, ls, 0.f, 0.f}, T);
      asm volatile("" : "+v"(sc[0]), "+v"(sc[1]), "+v"(sc[2]), "+v"(sc[3]), "+v"(sgr)); }
    __builtin_amdgcn_s_setprio(3);
    lds_wait_ge(L, F_PART + 3, 4u);
    { const LAS v2f* pp = (const LAS v2f*)(L + L_PART + j * 3072) + (lane & 31) * 3;
      unsigned u[3];
#pragma unroll
      for (int i = 0; i < 3; ++i) { const v2f o = ((sc[0] * pp[i] + sc[1] * pp[96 + i]) + sc[2] * pp[192 + i]) + sc[3] * pp[288 + i]; u[i] = pk(o.x, o.y); }
      __builtin_amdgcn_raw_buffer_store_b128(lane < 32 ? v4u{u[0], u[1], u[2], T.lo} : sgr, rws, lane < 33 ? lane * 16 : MASKED, WS_OP + ((j * 32 + h) * 8 + m) * 1024, SC1); }
    { v4u g[4]; float wk;
      const int hf = lane >> 5, p8 = lane & 7, poff = hf * 1024 + (lane & 31) * 16, soff = (2 * (p8 & 3) + ((p8 >> 2) ^ hf)) * 1024 + 512;
#pragma unroll 1
      for (int i = 0; i < SPIN; ++i) {
        FENCE();
        const v4u gs = __builtin_amdgcn_raw_buffer_load_b128(rws, soff, WS_OP + (j * 32 + hs) * 8192, SC1) ^ T.t;
#pragma unroll
        for (int k = 0; k < 4; ++k) g[k] = __builtin_amdgcn_raw_buffer_load_b128(rws, poff, WS_OP + (j * 32 + hs) * 8192 + k * 2048, SC1);
        SB();
        const float Mk = asf(gs.x);
        float Mg = fmaxf(Mk, dpp<0xB1>(Mk)); Mg = fmaxf(Mg, dpp<0x4E>(Mg)); Mg = fmaxf(Mg, dpp<0x141>(Mg));
        wk = exp2_((Mk - Mg) * LOG2E);
        float Lg = wk * asf(gs.y);
        Lg += dpp<0xB1>(Lg); Lg += dpp<0x4E>(Lg); Lg += dpp<0x141>(Lg);
        wk *= recip(Lg);
        asm volatile("" : "+v"(wk));
        SB();
        if (__all((((g[0].w ^ T.lo) | (g[1].w ^ T.lo)) | ((g[2].w ^ T.lo) | (g[3].w ^ T.lo)) | (or4(gs) & 1023u)) == 0u)) break; }
      const float w4[4] = {dpp<0x150>(wk), dpp<0x151>(wk), dpp<0x152>(wk), dpp<0x153>(wk)};
      v2f o[3] = {{0.f, 0.f}, {0.f, 0.f}, {0.f, 0.f}};
#pragma unroll
      for (int k = 0; k < 4; ++k)
#pragma unroll
        for (int i = 0; i < 3; ++i) o[i] += w4[k] * v2f{lo16(g[k][i]), hi16(g[k][i])};
      const float oa = sw32(o[0].x, o[1].x), ob2 = sw32(o[0].y, o[1].y), oc = sw32(o[2].x, o[2].y);
      LAS unsigned char* ob = L + L_OB + j * 384;
      *(LAS unsigned*)(ob + (lane & 31) * 12 + hf * 4) = pk(oa, ob2);
      *(LAS __bf16*)(ob + (lane & 31) * 12 + 8 + hf * 2) = (__bf16)oc;
      v4f y[5];
#pragma unroll
      for (int t = 0; t < 5; ++t) y[t] = v4f{0.f, 0.f, 0.f, 0.f};
#pragma unroll
      for (int s = 0; s < 6; ++s) {
        const v8b of = *(const LAS v8b*)(ob + s * 64 + qd * 16);
#pragma unroll
        for (int t = 0; t < 5; ++t) y[t] = __builtin_amdgcn_mfma_f32_16x16x32_bf16(of, wf[s][t], y[t], 0, 0, 0); }
      const v4f y4 = {dpp<0x00>(y[4].x), dpp<0x55>(y[4].x), dpp<0xAA>(y[4].x), dpp<0xFF>(y[4].x)};
      const v4f ys = qd == 0 ? v4f{y[0].x, y[1].x, y[2].x, y[3].x} : y4;
      __builtin_amdgcn_raw_buffer_store_b128(pack4(ys, T), rws, qd == 0 ? c * 16 : (qd == 1 && (c & 3) == 0) ? 256 + c * 4 : MASKED, WS_YP + (j * 32 + hs) * 2560 + m * 320, SC1); }
    if (reducer) {
      const int yoff = (lane >> 2) * 2560 + (lane & 3) * 16;
      v4u g0, g1;
#pragma unroll 1
      for (int i = 0; i < SPIN; ++i) {
        FENCE();
        g0 = __builtin_amdgcn_raw_buffer_load_b128(rws, yoff, WS_YP + j * 32 * 2560 + rj * 64, SC1) ^ T.t;
        g1 = __builtin_amdgcn_raw_buffer_load_b128(rws, yoff, WS_YP + (j * 32 + 16) * 2560 + rj * 64, SC1) ^ T.t;
        if (__all(((or4(g0) | or4(g1)) & 1023u) == 0u)) break; }
      const v4f y4 = __builtin_bit_cast(v4f, g0) + __builtin_bit_cast(v4f, g1);
      float y = sw16(sw32(y4.x, y4.z), sw32(y4.y, y4.w));
      y += dpp<0x124>(y); y += dpp<0x128>(y);
      const float hv = rbf(xv + rbf(y));
      float s1 = hv, s2 = hv * hv;
      s1 += dpp<0xB1>(s1); s2 += dpp<0xB1>(s2); s1 += dpp<0x4E>(s1); s2 += dpp<0x4E>(s2);
      s1 = sw16(s1, s1); s2 = sw16(s2, s2); s1 = sw32(s1, s1); s2 = sw32(s2, s2);
      __builtin_amdgcn_raw_buffer_store_b128(v4u{asu(s1), asu(s2), T.lo, T.hi}, rws, lane < NREP ? lane * 640 : MASKED, WS_SP + j * NREP * 640 + rj * 16, SC1);
      const bool hot = __builtin_amdgcn_readfirstlane(fl[F_HOT]) != 0u;
      v4u sg;
#pragma unroll 1
      for (int i = 0; i < SPIN; ++i) {
        FENCE();
        sg = __builtin_amdgcn_raw_buffer_load_b128(rws, lane < NRED ? lane * 16 : MASKED, WS_SP + (j * NREP + (wg & (NREP - 1))) * 640, SC1);
        if (__all((sg.z == T.lo && sg.w == T.hi) || lane >= NRED)) break; }
      const float mean = wave_sum(asf(sg.x)) * (1.f / 640.f), var = fmaxf(fmaf(wave_sum(asf(sg.y)), 1.f / 640.f, -mean * mean), 0.f);
      float m1, r1, m2, r2;
      ln_consts(mean, var, m1, r1);
      ln_consts((mean - m1) * r1, var * r1 * r1, m2, r2);
      const float tn = rbf(rbf(rbf(rbf(hv - m1) * r1) - m2) * r2);
      const unsigned tb = asu(tn) >> 16, mag = (tb & 0x7fffu) - TB_LO, e = min(mag, TB_N - 1u) + (tb >> 15) * TB_N, e7 = (e * 9363u) >> 16;
      float yv = lo16(*(const LAS unsigned short*)(L + L_TB + (e + e7) * 2));
      const bool own = (lane & 12) == 0;
      unsigned long long miss = __ballot(own && (mag >= TB_N || !hot));
#pragma unroll 1
      for (; miss; miss &= miss - 1ull) {
        const int src = __builtin_ctzll(miss);
        const float t = rdl(tn, src);
        v2f ac = {0.f, 0.f};
#pragma unroll 4
        for (int i = 0; i < 12; ++i) { const LAS v2f* mw = (const LAS v2f*)(L + L_MLP) + i * 64 + lane; ac += gelu2(t * mw[0] + mw[768]) * mw[1536]; }
        const float yd = rbf(rbf(wave_sum(ac.x + ac.y)) + b2);
        yv = lane == src ? yd : yv; }
      if (own) ((__bf16*)a.out)[j * 640 + rcol] = (__bf16)(hv + yv);
      if (wg == 0 && lane == 0) __builtin_amdgcn_raw_buffer_store_b64(v2u{T.lo, T.hi}, rws, 0, 0, SC1); } }
}

extern "C" void kernel_launch(void* const* d_in, const int* in_sizes, int n_in, void* d_out, int out_size, void* d_ws, size_t ws_size, hipStream_t stream) {
  Args a{d_in[0], d_in[1], d_in[2], d_in[3], d_in[4], d_in[6], d_in[7], d_in[8], d_in[9], d_out, d_ws};
  fused_decode<<<256, 512, 0, stream>>>(a);
}
```

```cpp
#include <hip/hip_runtime.h>
#include <hip/hip_bf16.h>


#define LAS __attribute__((address_space(3)))
typedef unsigned v4u __attribute__((ext_vector_type(4)));
typedef unsigned v2u __attribute__((ext_vector_type(2)));
typedef float v4f __attribute__((ext_vector_type(4)));
typedef float v2f __attribute__((ext_vector_type(2)));
typedef short v4s __attribute__((ext_vector_type(4)));
typedef __bf16 v8b __attribute__((ext_vector_type(8)));
typedef __bf16 v2b __attribute__((ext_vector_type(2)));
typedef __amdgpu_buffer_rsrc_t rsrc_t;

namespace {
constexpr int LLO = 5, LMID = 7, LMAX = 9, NPRE = 8;
constexpr int BLO = 20, BHI = 40, TAUB = 248, DLY = 200, CONV = 124;
constexpr int NREP = 8, CSTR = 256, NRED = 40;
constexpr unsigned SLAB = 4096u * 384u, KVB = 128u * SLAB;
constexpr unsigned WS_QP = 4096, WS_OP = WS_QP + 256 * 1024, WS_YP = WS_OP + 4 * 256 * 1024, WS_TB = WS_YP + 128 * 2560, WS_SP = WS_TB + 256 * 64,
                   WS_CP = WS_SP + 4 * NREP * 640, WS_END = WS_CP + 3 * 32 * CSTR;
constexpr int L_Q = 0, L_FLAG = 1536, L_ML = 1664, L_OB = 1792, L_RED = L_OB + 4 * 384, L_PART = L_RED + 512, L_MLP = L_PART + 16 * 768, L_STG = L_MLP + 3 * 6144,
              L_TB = L_STG + 4 * 7680, L_END = L_TB + 16384;
constexpr int F_PART = 0, F_Q = 4, F_TB = 8, F_WO = 9, F_STEP = 10, F_HOT = 11, F_TAU = 12, F_BASE = 13, F_TIME = 16, F_ML = 24, F_QS = 28;
constexpr unsigned TB_LO = 102u * 128u, TB_N = 28u * 128u;
constexpr int MASKED = 0x40000000, NT = 2, SC1 = 16;
constexpr int SPIN = 1 << 15;
constexpr float LOG2E = 1.4426950408889634f;

__device__ __forceinline__ rsrc_t mk(const void* p, unsigned n) { return __builtin_amdgcn_make_buffer_rsrc((void*)p, 0, (int)n, 0x00020000); }
__device__ __forceinline__ float asf(unsigned u) { return __builtin_bit_cast(float, u); }
__device__ __forceinline__ unsigned asu(float f) { return __builtin_bit_cast(unsigned, f); }
__device__ __forceinline__ float lo16(unsigned u) { return asf(u << 16); }
__device__ __forceinline__ float hi16(unsigned u) { return asf(u & 0xffff0000u); }
__device__ __forceinline__ unsigned pk(float a, float b) { return __builtin_bit_cast(unsigned, v2b{(__bf16)a, (__bf16)b}); }
__device__ __forceinline__ float rbf(float x) { return (float)(__bf16)x; }
__device__ __forceinline__ float dot2(unsigned a, unsigned b, float c) {
  return __builtin_amdgcn_fdot2_f32_bf16(__builtin_bit_cast(v2b, a), __builtin_bit_cast(v2b, b), c, false); }
template <int C> __device__ __forceinline__ float dpp(float x) { return asf(__builtin_amdgcn_mov_dpp(asu(x), C, 0xf, 0xf, true)); }
__device__ __forceinline__ float sw16(float a, float b) { auto r = __builtin_amdgcn_permlane16_swap(asu(a), asu(b), false, false); return asf(r[0]) + asf(r[1]); }
__device__ __forceinline__ float sw32(float a, float b) { auto r = __builtin_amdgcn_permlane32_swap(asu(a), asu(b), false, false); return asf(r[0]) + asf(r[1]); }
__device__ __forceinline__ float row_sum(float x) { x += dpp<0x128>(x); x += dpp<0x124>(x); x += dpp<0x122>(x); x += dpp<0x121>(x); return x; }
__device__ __forceinline__ float wave_sum(float x) { x = row_sum(x); x = sw16(x, x); return sw32(x, x); }
__device__ __forceinline__ float exp2_(float x) { return __builtin_amdgcn_exp2f(x); }
__device__ __forceinline__ float recip(float x) { const float r = __builtin_amdgcn_rcpf(x); return fmaf(fmaf(-x, r, 1.f), r, r); }
__device__ __forceinline__ float rdl(float x, int l) { return asf(__builtin_amdgcn_readlane(asu(x), l)); }
__device__ __forceinline__ unsigned clock10() { return (unsigned)__builtin_amdgcn_s_memrealtime(); }
__device__ __forceinline__ void ln_consts(float mean, float var, float& m, float& rinv) {
  m = rbf(mean); rinv = recip(rbf(__builtin_amdgcn_sqrtf(rbf(rbf(var) + 1e-5f)))); }
__device__ __forceinline__ v2f gelu2(v2f u) {
  const v2f z = u * (u * u * (-0.044715f * 1.5957691216057308f * LOG2E) + (-1.5957691216057308f * LOG2E));
  const v2f d = v2f{exp2_(z.x), exp2_(z.y)} + 1.f;
  return u * v2f{__builtin_amdgcn_rcpf(d.x), __builtin_amdgcn_rcpf(d.y)}; }

struct Tag { unsigned lo, hi; v4u t; };
__device__ __forceinline__ Tag make_tag(unsigned long long e) {
  const unsigned long long t = e + 1ull;
  return Tag{(unsigned)t, (unsigned)(t >> 32), v4u{(unsigned)t & 1023u, (unsigned)(t >> 10) & 1023u, (unsigned)(t >> 20) & 1023u, (unsigned)(t >> 30) & 1023u}}; }
__device__ __forceinline__ unsigned bfi(unsigned m, unsigned a, unsigned b) { unsigned r; asm("v_bfi_b32 %0, %1, %2, %3" : "=v"(r) : "v"(m), "s"(a), "v"(b)); return r; }
__device__ __forceinline__ v4u pack4(v4f v, const Tag& T) {
  return v4u{bfi(1023u, T.t.x, asu(v.x) + 512u), bfi(1023u, T.t.y, asu(v.y) + 512u), bfi(1023u, T.t.z, asu(v.z) + 512u), bfi(1023u, T.t.w, asu(v.w) + 512u)}; }
__device__ __forceinline__ unsigned or4(v4u x) { return (x.x | x.y) | (x.z | x.w); }

#define FENCE() asm volatile("" ::: "memory")
#define SB() __builtin_amdgcn_sched_barrier(0)
#define LDS_WAIT() asm volatile("s_waitcnt lgkmcnt(0)" ::: "memory")
#define BARRIER() asm volatile("s_waitcnt lgkmcnt(0)\n\ts_barrier" ::: "memory")
#define VM_WAIT(n) do { asm volatile("s_waitcnt vmcnt(%0)" ::"n"(n) : "memory"); SB(); } while (0)

template <int SLEEP = 0> __device__ __forceinline__ void lds_wait_ge(LAS unsigned char* L, int word, unsigned want) {
  volatile LAS unsigned* p = (volatile LAS unsigned*)(L + L_FLAG) + word;
#pragma unroll 1
  for (int i = 0; i < (SPIN << 5); ++i) { if (__builtin_amdgcn_readfirstlane(*p) >= want) break; if (SLEEP) __builtin_amdgcn_s_sleep(SLEEP); }
  FENCE(); }
__device__ __forceinline__ void lds_add(LAS unsigned char* L, int word, int lane) {
  LDS_WAIT();
  if (lane == 0) __hip_atomic_fetch_add((LAS unsigned*)(L + L_FLAG) + word, 1u, __ATOMIC_RELAXED, __HIP_MEMORY_SCOPE_WORKGROUP); }
}

struct Args { const void *x, *k, *v, *wq, *wo, *w1, *b1, *w2, *b2; void *out, *ws; };

__global__ void __launch_bounds__(512, 2) fused_decode(Args a) {
  __shared__ __attribute__((aligned(16))) unsigned char lds_raw[L_END];
  LAS unsigned char* L = (LAS unsigned char*)lds_raw;
  volatile LAS unsigned* fl = (volatile LAS unsigned*)(L + L_FLAG);
  const int wg = blockIdx.x, lane = threadIdx.x & 63, h = wg & 31, m = wg >> 5, hs = (h + 1) & 31;
  const int wave = __builtin_amdgcn_readfirstlane(threadIdx.x >> 6);
  asm volatile("" : "+s"(a.x), "+s"(a.k), "+s"(a.v), "+s"(a.wq), "+s"(a.ws));

  if (wave < 4) {
    const int w = wave;
    __builtin_amdgcn_s_setprio(2);
    const unsigned loff = (lane >> 3) * 384 + (lane & 7) * 16;
    const unsigned wbase = h * SLAB;
    unsigned go[4];
#pragma unroll
    for (int r = 0; r < 4; ++r) { const int p = r * 32 + w * 8 + m, q = p < 43 ? p : p < 86 ? p - 43 : p - 86;
      go[r] = ((q >> 1) + (p < 43 ? ((q & 1) ? 22 : 0) : p < 86 ? ((q & 1) ? 43 : 64) : ((q & 1) ? 86 : 107))) * 3072; }
    const rsrc_t rk = mk(a.k, KVB), rv = mk(a.v, KVB);
    v4u ring[4][3], pre[NPRE][3];
    auto ld = [&](const rsrc_t& rs, unsigned so, int n) { return __builtin_amdgcn_raw_buffer_load_b128(rs, loff + (n % 3) * 128, so + (n / 12) * 393216 + go[n / 3 % 4], NT); };
    auto issue = [&](int g, const rsrc_t& r0, unsigned s0, const rsrc_t& r1, unsigned s1) {
#pragma unroll
      for (int n = 3 * g + LMAX; n < 3 * g + LMAX + 3; ++n) ring[(n / 3) % 4][n % 3] = n < 48 ? ld(r0, s0, n) : ld(r1, s1, n - 48);
      SB(); };
    if (w == 0 && lane < 32) fl[lane] = 0u;
    BARRIER(); SB();
#pragma unroll
    for (int n = 0; n < 3 * NPRE + LMAX; ++n) {
      if (n >= LMID) VM_WAIT(LMID - 1);
      if (n < 3 * NPRE) pre[n / 3][n % 3] = ld(rk, wbase, n); else ring[(n / 3) % 4][n % 3] = ld(rk, wbase, n);
      SB(); }
    BARRIER();
    v4u q[3];
    float s[16];
    auto loadq = [&](int b) {
#pragma unroll
      for (int j = 0; j < 3; ++j) q[j] = *(const LAS v4u*)(L + L_Q + b * 384 + (lane & 7) * 16 + j * 128); };
    auto score = [&](const v4u(&r)[3]) -> float {
      float d0 = 0.f, d1 = 0.f, d2 = 0.f;
#pragma unroll
      for (int k = 0; k < 4; ++k) { d0 = dot2(r[0][k], q[0][k], d0); d1 = dot2(r[1][k], q[1][k], d1); d2 = dot2(r[2][k], q[2][k], d2); }
      float d = (d0 + d1) + d2;
      d += dpp<0xB1>(d); d += dpp<0x4E>(d); d += dpp<0x141>(d);
      d = rbf(rbf(d) * 0.07216878364870322f);
      asm volatile("" : "+v"(d));
      return d; };
    int mode = 1;
    auto wait_step = [&] { if (mode == 0) VM_WAIT(LLO - 3); else if (mode == 1) VM_WAIT(LMID - 3); else VM_WAIT(LMAX - 3); };
    unsigned now, tauv, basev;
    auto pace_a = [&](int g) { if ((g & 1) == 0) { now = clock10(); tauv = fl[F_TAU]; basev = fl[F_BASE]; SB(); } };
    auto pace_b = [&](int p, int g) { if ((g & 1) == 0) {
      const int n = p * 16 + g;
      if (w == 0 && lane == 0) { if (g == 0) fl[F_TIME + p] = now; fl[F_STEP] = n; }
      const int tau = __builtin_amdgcn_readfirstlane(tauv), lead = (int)((unsigned)__builtin_amdgcn_readfirstlane(basev) + (unsigned)((n * tau) >> 8) - now);
      mode = tau == 0 ? 1 : lead > BLO ? 0 : lead < -BHI ? 2 : 1; } };
    loadq(0);
#pragma unroll
    for (int g = NPRE; g < 16; ++g) {
      VM_WAIT(LMID - 3);
      issue(g, rk, wbase, rv, wbase);
      s[g] = score(ring[g % 4]);
      s[g - NPRE] = score(pre[g - NPRE]);
      SB(); }
    const int b3 = (lane >> 3) & 1, a0 = 12 * (lane >> 5) + 6 * ((lane >> 4) & 1) + 3 * b3;
#pragma unroll 1
    for (int b = 0;; ++b) {
      const unsigned sb = wbase + b * (32 * SLAB);
      const rsrc_t rn = mk(a.k, b < 3 ? KVB : 0u);
      const unsigned nb = b < 3 ? sb + 32 * SLAB : 0u;
      v2f ac2[12];
#pragma unroll
      for (int g = 0; g < 16; ++g) {
        wait_step();
        issue(g, rv, sb, rn, nb);
        pace_a(g);
        if (g == 0) {
          float mx = s[0], l = 0.f;
#pragma unroll
          for (int i = 1; i < 16; ++i) mx = fmaxf(mx, s[i]);
          mx = fmaxf(mx, dpp<0x128>(mx));
          { auto r = __builtin_amdgcn_permlane16_swap(asu(mx), asu(mx), false, false); mx = fmaxf(asf(r[0]), asf(r[1])); }
          { auto r = __builtin_amdgcn_permlane32_swap(asu(mx), asu(mx), false, false); mx = fmaxf(asf(r[0]), asf(r[1])); }
#pragma unroll
          for (int i = 0; i < 16; ++i) { s[i] = exp2_((s[i] - mx) * LOG2E); l += s[i]; }
          l += dpp<0x128>(l); l = sw16(l, l); l = sw32(l, l);
          if (lane == 0) *(LAS v2f*)(L + L_ML + (b * 4 + w) * 8) = v2f{mx, l};
          lds_add(L, F_ML + b, lane);
#pragma unroll
          for (int k = 0; k < 12; ++k) ac2[k] = v2f{0.f, 0.f}; }
#pragma unroll
        for (int k = 0; k < 12; ++k) { const unsigned u = ring[g % 4][k / 4][k % 4];
          ac2[k] += s[g] * v2f{lo16(u), hi16(u)};
          asm volatile("" : "+v"(ac2[k])); }
        pace_b(2 * b + 1, g);
        if (g >= 12 && b == 3) mode = 2;
        SB(); }
      float acc[12];
#pragma unroll
      for (int i = 0; i < 12; ++i) acc[i] = sw32(ac2[i / 2][i % 2], ac2[6 + i / 2][i % 2]);
#pragma unroll
      for (int i = 0; i < 6; ++i) acc[i] = sw16(acc[i], acc[i + 6]);
      LAS float* part = (LAS float*)(L + L_PART + (b * 4 + w) * 768);
#pragma unroll
      for (int t = 0; t < 3; ++t) { const float u0 = acc[t] + dpp<0x128>(acc[t]), u1 = acc[t + 3] + dpp<0x128>(acc[t + 3]);
        const int ai = a0 + t; part[64 * (ai >> 3) + 8 * (lane & 7) + (ai & 7)] = b3 ? u1 : u0; }
      lds_add(L, F_PART + b, lane);
      if (b == 3) break;
      lds_wait_ge(L, F_Q + b + 1, 1u);
      loadq(b + 1);
#pragma unroll
      for (int g = 0; g < 16; ++g) {
        wait_step();
        issue(g, rk, nb, rv, nb);
        pace_a(g);
        s[g] = score(ring[g % 4]);
        pace_b(2 * b + 2, g);
        SB(); }
    }
    { const rsrc_t rw = mk(a.ws, WS_END);
      unsigned z = __builtin_amdgcn_raw_buffer_load_b32(rw, lane < 5 ? lane * 64 : MASKED, WS_YP + (w * 32 + hs) * 2560 + m * 320, SC1);
      if (w == 1) z |= __builtin_amdgcn_raw_buffer_load_b32(rw, (wg & 63) < NRED && lane < 10 ? lane * 64 : MASKED, WS_SP + ((wg >> 6) * NREP + (wg & (NREP - 1))) * 640, SC1);
      asm volatile("" :: "v"(z)); }
  } else {
    const int j = wave - 4, c = lane & 15, qd = lane >> 4;
    const rsrc_t rws = mk(a.ws, WS_END), rx = mk(a.x, 5120);
    LAS unsigned char* stg = L + L_STG + j * 7680;
    unsigned long long e0 = *(const __attribute__((address_space(4))) unsigned long long*)a.ws;
    v4u xr[5], wr[8]; v2u xo[5];
#pragma unroll
    for (int i = 0; i < 5; ++i) xr[i] = __builtin_amdgcn_raw_buffer_load_b128(rx, qd * 1280 + c * 16 + i * 256, 0, 0);
#pragma unroll
    for (int i = 0; i < 5; ++i) xo[i] = __builtin_amdgcn_raw_buffer_load_b64(rx, (c >> 2) * 1280 + qd * 8 + i * 32, ((m >> 2) * 4 + j) * 160, 0);
    SB();
    { const rsrc_t rq = mk(a.wq, 640 * 12288);
      const int off = lane < 60 ? (lane / 6) * 12288 + (lane % 6) * 16 : MASKED;
#pragma unroll
      for (int i = 0; i < 8; ++i) wr[i] = __builtin_amdgcn_raw_buffer_load_b128(rq, off, (((m >> 2) * 4 + j) * 80 + i * 10) * 12288 + hs * 384 + (m & 3) * 96, NT); }
    SB();
    __builtin_amdgcn_s_barrier();
    SB(); FENCE();
    float sum = 0.f;
#pragma unroll
    for (int i = 0; i < 5; ++i)
#pragma unroll
      for (int k = 0; k < 4; ++k) sum += lo16(xr[i][k]) + hi16(xr[i][k]);
    const float mean0 = row_sum(sum) * (1.f / 640.f);
    float sq = 0.f;
#pragma unroll
    for (int i = 0; i < 5; ++i)
#pragma unroll
      for (int k = 0; k < 4; ++k) { const float d0 = lo16(xr[i][k]) - mean0, d1 = hi16(xr[i][k]) - mean0; sq = fmaf(d0, d0, fmaf(d1, d1, sq)); }
    float mm, rinv; ln_consts(mean0, row_sum(sq) * (1.f / 640.f), mm, rinv);
    { const int src = (c >> 2) * 64; mm = asf(__builtin_amdgcn_ds_bpermute(src, asu(mm))); rinv = asf(__builtin_amdgcn_ds_bpermute(src, asu(rinv))); }
    v4s xb[5];
#pragma unroll
    for (int i = 0; i < 5; ++i) {
      xb[i] = __builtin_bit_cast(v4s, v2u{pk(rbf(lo16(xo[i].x) - mm) * rinv, rbf(hi16(xo[i].x) - mm) * rinv), pk(rbf(lo16(xo[i].y) - mm) * rinv, rbf(hi16(xo[i].y) - mm) * rinv)});
      asm volatile("" : "+v"(xb[i])); }
    asm volatile("" : "+s"(e0));
    SB();
    const Tag T = make_tag(e0);
    if (lane < 60) {
#pragma unroll
      for (int i = 0; i < 8; ++i) *(LAS v4u*)(stg + i * 960 + lane * 16) = wr[i]; }
    { const int troff = (4 * qd + (c >> 2)) * 96 + (c & 3) * 8;
      v4f d[3];
#pragma unroll
      for (int t = 0; t < 3; ++t) {
        d[t] = v4f{0.f, 0.f, 0.f, 0.f};
#pragma unroll
        for (int i = 0; i < 5; ++i)
          d[t] = __builtin_amdgcn_mfma_f32_16x16x16bf16_1k(xb[i], __builtin_amdgcn_ds_read_tr16_b64_v4i16((LAS v4s*)(stg + troff + i * 1536 + t * 32)), d[t], 0, 0, 0); }
      LAS v4f* qp = (LAS v4f*)(L + L_PART);
      qp[j * 64 + lane] = v4f{d[0].x, d[1].x, d[2].x, 0.f};
      lds_add(L, F_QS, lane);
      if (j == 0) {
        lds_wait_ge(L, F_QS, 4u);
        const v4f t = (qp[lane] + qp[64 + lane]) + (qp[128 + lane] + qp[192 + lane]);
        __builtin_amdgcn_raw_buffer_store_b128(v4u{asu(t.x), asu(t.y), asu(t.z), T.lo}, rws, lane * 16, WS_QP + (hs * 8 + m) * 1024, SC1); } }
    if (j > 0) { BARRIER(); __builtin_amdgcn_s_sleep(8); } else __builtin_amdgcn_s_sleep(40);
    { v4u g0, g1;
#pragma unroll 1
      for (int i = 0; i < SPIN; ++i) {
        FENCE();
        g0 = __builtin_amdgcn_raw_buffer_load_b128(rws, qd * 1024 + c * 16, WS_QP + h * 8192 + j * 256, SC1);
        g1 = __builtin_amdgcn_raw_buffer_load_b128(rws, qd * 1024 + c * 16, WS_QP + h * 8192 + 4096 + j * 256, SC1);
        if (__all(g0.w == T.lo && g1.w == T.lo)) break;
        __builtin_amdgcn_s_sleep(8); }
      LAS __bf16* qs = (LAS __bf16*)(L + L_Q + j * 384) + 48 * qd + c;
      qs[0] = (__bf16)(asf(g0.x) + asf(g1.x)); qs[16] = (__bf16)(asf(g0.y) + asf(g1.y)); qs[32] = (__bf16)(asf(g0.z) + asf(g1.z)); }
    if (j == 0) BARRIER(); else lds_add(L, F_Q + j, lane);
    const float b2 = lo16(*(const unsigned short*)a.b2);
    if (j < 3) {
      { const rsrc_t r1 = mk(a.w1, 3072), r2 = mk(a.b1, 3072), r3 = mk(a.w2, 3072);
        unsigned mr[12];
#pragma unroll
        for (int i = 0; i < 4; ++i) { mr[i] = __builtin_amdgcn_raw_buffer_load_b32(r1, lane * 4 + i * 256, j * 1024, 0);
          mr[4 + i] = __builtin_amdgcn_raw_buffer_load_b32(r2, lane * 4 + i * 256, j * 1024, 0);
          mr[8 + i] = __builtin_amdgcn_raw_buffer_load_b32(r3, lane * 4 + i * 256, j * 1024, 0); }
#pragma unroll
        for (int i = 0; i < 12; ++i) *(LAS v2f*)(L + L_MLP + (i / 4) * 6144 + j * 2048 + (i % 4) * 512 + lane * 8) = v2f{lo16(mr[i]), hi16(mr[i])}; }
      const unsigned e = wg * 28 + min(c, 13) * 2, neg = e >= TB_N, bits = (e - neg * TB_N + TB_LO) | (neg << 15);
      const v2f t2 = {lo16(bits), lo16(bits + 1u)};
      v2f ac = {0.f, 0.f};
      const LAS v4f* mw = (const LAS v4f*)(L + L_MLP + j * 2048 + qd * 512);
#pragma unroll 2
      for (int i = 0; i < 32; ++i) {
        const v4f w1 = mw[i], b1 = mw[384 + i], w2 = mw[768 + i];
#pragma unroll
        for (int k = 0; k < 4; ++k) ac += gelu2(t2 * w1[k] + b1[k]) * w2[k]; }
      ac.x = sw16(ac.x, ac.x); ac.y = sw16(ac.y, ac.y); ac.x = sw32(ac.x, ac.x); ac.y = sw32(ac.y, ac.y);
      LAS v2f* red = (LAS v2f*)(L + L_RED);
      if (lane < 16) red[j * 16 + lane] = ac;
      lds_add(L, F_TB, lane);
      if (j == 0) {
        lds_wait_ge<1>(L, F_TB, 3u);
        const v2f y = (red[c] + red[16 + c]) + red[32 + c];
        LAS unsigned short* tg = (LAS unsigned short*)(L + L_RED + 384);
        const unsigned yb = pk(rbf(y.x) + b2, rbf(y.y) + b2);
        if (lane < 14) { tg[(2 * c) / 7 * 8 + (2 * c) % 7] = (unsigned short)yb; tg[(2 * c + 1) / 7 * 8 + (2 * c + 1) % 7] = (unsigned short)(yb >> 16); }
        if (lane < 4) tg[lane * 8 + 7] = (unsigned short)T.lo;
        __builtin_amdgcn_raw_buffer_store_b128(*(const LAS v4u*)(tg + (lane & 3) * 8), rws, lane < 4 ? lane * 16 : MASKED, WS_TB + wg * 64, SC1); }
      { const rsrc_t ro = mk(a.wo, 6144 * 1280);
        const int off = lane < 60 ? (lane / 10) * 1280 + (lane % 10) * 16 : MASKED;
#pragma unroll 1
        for (int p = j; p < 32; p += 3) {
          lds_wait_ge<8>(L, F_STEP, 24u + 2u * p);
          const v4u t = __builtin_amdgcn_raw_buffer_load_b128(ro, off, (hs * 192 + p * 6) * 1280 + m * 160, NT);
          if (lane < 60) *(LAS v4u*)(L + L_STG + p * 960 + lane * 16) = t; }
        lds_add(L, F_WO, lane); }
      if (j == 2 && (wg & 63) < NRED) {
        unsigned bad = 0;
#pragma unroll 1
        for (int p = 0; p < 16; ++p) {
          lds_wait_ge<8>(L, F_STEP, 88u + p);
          const v4u t = __builtin_amdgcn_raw_buffer_load_b128(rws, lane * 16, WS_TB + p * 1024, SC1);
          bad |= (t.w >> 16) ^ (T.lo & 0xffffu);
          *(LAS v4u*)(L + L_TB + p * 1024 + lane * 16) = t; }
        LDS_WAIT();
        if (__all(bad == 0u) && lane == 0) fl[F_HOT] = 1u; }
    } else {
      unsigned prev = 0;
#pragma unroll 1
      for (int k = 0; k < 3; ++k) {
        const int n0 = 32 * (k + 1);
        lds_wait_ge<2>(L, F_STEP, n0);
        const unsigned own = __builtin_amdgcn_readfirstlane(fl[F_TIME + 2 * k + 2]);
        __builtin_amdgcn_raw_buffer_store_b64(v2u{own, T.lo}, rws, lane == 0 ? (wg >> 3) * CSTR + (wg & 7) * 8 : MASKED, WS_CP + k * 32 * CSTR, SC1);
        float sum = 0.f; int cnt = 0;
#pragma unroll 1
        for (int tries = 0; tries < 2 && cnt < 32; ++tries) {
#pragma unroll 1
          for (int it = 0; it < SPIN; ++it) { if ((int)(clock10() - own) >= DLY + 60 * tries) break; __builtin_amdgcn_s_sleep(4); }
          FENCE();
          const v2u e = __builtin_amdgcn_raw_buffer_load_b64(rws, lane < 32 ? (((wg >> 3) + (lane & 24)) & 31) * CSTR + (lane & 7) * 8 : MASKED, WS_CP + k * 32 * CSTR, SC1);
          const bool ok = lane < 32 && e.y == T.lo;
          cnt = __builtin_popcountll(__ballot(ok));
          sum = wave_sum(ok ? (float)(int)(e.x - own) : 0.f); }
        const float lag = cnt ? sum * __builtin_amdgcn_rcpf((float)cnt) : 0.f;
        const unsigned mean = own + (unsigned)__builtin_amdgcn_readfirstlane((int)lag);
        if (k > 0) { const int tau = (((int)(mean - prev) * TAUB) >> 5) + (int)(lag * 256.f * __builtin_amdgcn_rcpf((float)(CONV - n0)));
          if (lane == 0) { fl[F_BASE] = own - (unsigned)((n0 * tau) >> 8); fl[F_TAU] = tau; } }
        prev = mean; } }
    v8b wf[6][5];
    { lds_wait_ge<4>(L, F_WO, 3u);
      const int tro = L_STG + (8 * qd + (c >> 2)) * 160 + (c & 3) * 8;
#pragma unroll
      for (int s = 0; s < 6; ++s)
#pragma unroll
        for (int t = 0; t < 5; ++t) {
          const v4s p0 = __builtin_amdgcn_ds_read_tr16_b64_v4i16((LAS v4s*)(L + tro + s * 5120 + t * 32));
          const v4s p1 = __builtin_amdgcn_ds_read_tr16_b64_v4i16((LAS v4s*)(L + tro + s * 5120 + t * 32 + 640));
          wf[s][t] = __builtin_bit_cast(v8b, __builtin_shufflevector(p0, p1, 0, 1, 2, 3, 4, 5, 6, 7)); }
#pragma unroll
      for (int s = 0; s < 6; ++s) asm volatile("" : "+v"(wf[s][0]), "+v"(wf[s][1]), "+v"(wf[s][2]), "+v"(wf[s][3]), "+v"(wf[s][4])); }
    const int rj = wg - 64 * j, rm = (rj & 63) / 5, rs5 = (rj & 63) - 5 * rm, rcol = 80 * rm + (rs5 < 4 ? 16 * qd + 4 * rs5 + (lane & 3) : 64 + 4 * (lane & 3) + qd);
    const bool reducer = rj >= 0 && rj < NRED;
    const float xv = lo16(__builtin_amdgcn_raw_buffer_load_b16(rx, reducer ? rcol * 2 : MASKED, j * 1280, 0));
    float sc[4], M, ls; v4u sgr;
    { lds_wait_ge<4>(L, F_ML + j, 4u);
      const v4f p0 = *(const LAS v4f*)(L + L_ML + j * 32), p1 = *(const LAS v4f*)(L + L_ML + j * 32 + 16);
      M = fmaxf(fmaxf(p0.x, p0.z), fmaxf(p1.x, p1.z));
      sc[0] = exp2_((p0.x - M) * LOG2E); sc[1] = exp2_((p0.z - M) * LOG2E); sc[2] = exp2_((p1.x - M) * LOG2E); sc[3] = exp2_((p1.z - M) * LOG2E);
      ls = fmaf(sc[3], p1.w, fmaf(sc[2], p1.y, fmaf(sc[1], p0.w, sc[0] * p0.y)));
      sgr = pack4(v4f{M, ls, 0.f, 0.f}, T);
      asm volatile("" : "+v"(sc[0]), "+v"(sc[1]), "+v"(sc[2]), "+v"(sc[3]), "+v"(sgr)); }
    __builtin_amdgcn_s_setprio(3);
    lds_wait_ge(L, F_PART + 3, 4u);
    { const LAS v2f* pp = (const LAS v2f*)(L + L_PART + j * 3072) + (lane & 31) * 3;
      unsigned u[3];
#pragma unroll
      for (int i = 0; i < 3; ++i) { const v2f o = ((sc[0] * pp[i] + sc[1] * pp[96 + i]) + sc[2] * pp[192 + i]) + sc[3] * pp[288 + i]; u[i] = pk(o.x, o.y); }
      __builtin_amdgcn_raw_buffer_store_b128(lane < 32 ? v4u{u[0], u[1], u[2], T.lo} : sgr, rws, lane < 33 ? lane * 16 : MASKED, WS_OP + ((j * 32 + h) * 8 + m) * 1024, SC1); }
    { v4u g[4]; float wk;
      const int hf = lane >> 5, p8 = lane & 7, poff = hf * 1024 + (lane & 31) * 16, soff = (2 * (p8 & 3) + ((p8 >> 2) ^ hf)) * 1024 + 512;
#pragma unroll 1
      for (int i = 0; i < SPIN; ++i) {
        FENCE();
        const v4u gs = __builtin_amdgcn_raw_buffer_load_b128(rws, soff, WS_OP + (j * 32 + hs) * 8192, SC1) ^ T.t;
#pragma unroll
        for (int k = 0; k < 4; ++k) g[k] = __builtin_amdgcn_raw_buffer_load_b128(rws, poff, WS_OP + (j * 32 + hs) * 8192 + k * 2048, SC1);
        SB();
        const float Mk = asf(gs.x);
        float Mg = fmaxf(Mk, dpp<0xB1>(Mk)); Mg = fmaxf(Mg, dpp<0x4E>(Mg)); Mg = fmaxf(Mg, dpp<0x141>(Mg));
        wk = exp2_((Mk - Mg) * LOG2E);
        float Lg = wk * asf(gs.y);
        Lg += dpp<0xB1>(Lg); Lg += dpp<0x4E>(Lg); Lg += dpp<0x141>(Lg);
        wk *= recip(Lg);
        asm volatile("" : "+v"(wk));
        SB();
        if (__all((((g[0].w ^ T.lo) | (g[1].w ^ T.lo)) | ((g[2].w ^ T.lo) | (g[3].w ^ T.lo)) | (or4(gs) & 1023u)) == 0u)) break; }
      const float w4[4] = {dpp<0x150>(wk), dpp<0x151>(wk), dpp<0x152>(wk), dpp<0x153>(wk)};
      v2f o[3] = {{0.f, 0.f}, {0.f, 0.f}, {0.f, 0.f}};
#pragma unroll
      for (int k = 0; k < 4; ++k)
#pragma unroll
        for (int i = 0; i < 3; ++i) o[i] += w4[k] * v2f{lo16(g[k][i]), hi16(g[k][i])};
      const float oa = sw32(o[0].x, o[1].x), ob2 = sw32(o[0].y, o[1].y), oc = sw32(o[2].x, o[2].y);
      LAS unsigned char* ob = L + L_OB + j * 384;
      *(LAS unsigned*)(ob + (lane & 31) * 12 + hf * 4) = pk(oa, ob2);
      *(LAS __bf16*)(ob + (lane & 31) * 12 + 8 + hf * 2) = (__bf16)oc;
      v4f y[5];
#pragma unroll
      for (int t = 0; t < 5; ++t) y[t] = v4f{0.f, 0.f, 0.f, 0.f};
#pragma unroll
      for (int s = 0; s < 6; ++s) {
        const v8b of = *(const LAS v8b*)(ob + s * 64 + qd * 16);
#pragma unroll
        for (int t = 0; t < 5; ++t) y[t] = __builtin_amdgcn_mfma_f32_16x16x32_bf16(of, wf[s][t], y[t], 0, 0, 0); }
      const v4f y4 = {dpp<0x00>(y[4].x), dpp<0x55>(y[4].x), dpp<0xAA>(y[4].x), dpp<0xFF>(y[4].x)};
      const v4f ys = qd == 0 ? v4f{y[0].x, y[1].x, y[2].x, y[3].x} : y4;
      __builtin_amdgcn_raw_buffer_store_b128(pack4(ys, T), rws, qd == 0 ? c * 16 : (qd == 1 && (c & 3) == 0) ? 256 + c * 4 : MASKED, WS_YP + (j * 32 + hs) * 2560 + m * 320, SC1); }
    if (reducer) {
      const int yoff = (lane >> 2) * 2560 + (lane & 3) * 16;
      v4u g0, g1;
#pragma unroll 1
      for (int i = 0; i < SPIN; ++i) {
        FENCE();
        g0 = __builtin_amdgcn_raw_buffer_load_b128(rws, yoff, WS_YP + j * 32 * 2560 + rj * 64, SC1) ^ T.t;
        g1 = __builtin_amdgcn_raw_buffer_load_b128(rws, yoff, WS_YP + (j * 32 + 16) * 2560 + rj * 64, SC1) ^ T.t;
        if (__all(((or4(g0) | or4(g1)) & 1023u) == 0u)) break; }
      const v4f y4 = __builtin_bit_cast(v4f, g0) + __builtin_bit_cast(v4f, g1);
      float y = sw16(sw32(y4.x, y4.z), sw32(y4.y, y4.w));
      y += dpp<0x124>(y); y += dpp<0x128>(y);
      const float hv = rbf(xv + rbf(y));
      float s1 = hv, s2 = hv * hv;
      s1 += dpp<0xB1>(s1); s2 += dpp<0xB1>(s2); s1 += dpp<0x4E>(s1); s2 += dpp<0x4E>(s2);
      s1 = sw16(s1, s1); s2 = sw16(s2, s2); s1 = sw32(s1, s1); s2 = sw32(s2, s2);
      __builtin_amdgcn_raw_buffer_store_b128(v4u{asu(s1), asu(s2), T.lo, T.hi}, rws, lane < NREP ? lane * 640 : MASKED, WS_SP + j * NREP * 640 + rj * 16, SC1);
      const bool hot = __builtin_amdgcn_readfirstlane(fl[F_HOT]) != 0u;
      v4u sg;
#pragma unroll 1
      for (int i = 0; i < SPIN; ++i) {
        FENCE();
        sg = __builtin_amdgcn_raw_buffer_load_b128(rws, lane < NRED ? lane * 16 : MASKED, WS_SP + (j * NREP + (wg & (NREP - 1))) * 640, SC1);
        if (__all((sg.z == T.lo && sg.w == T.hi) || lane >= NRED)) break; }
      const float mean = wave_sum(asf(sg.x)) * (1.f / 640.f), var = fmaxf(fmaf(wave_sum(asf(sg.y)), 1.f / 640.f, -mean * mean), 0.f);
      float m1, r1, m2, r2;
      ln_consts(mean, var, m1, r1);
      ln_consts((mean - m1) * r1, var * r1 * r1, m2, r2);
      const float tn = rbf(rbf(rbf(rbf(hv - m1) * r1) - m2) * r2);
      const unsigned tb = asu(tn) >> 16, mag = (tb & 0x7fffu) - TB_LO, e = min(mag, TB_N - 1u) + (tb >> 15) * TB_N, e7 = (e * 9363u) >> 16;
      float yv = lo16(*(const LAS unsigned short*)(L + L_TB + (e + e7) * 2));
      const bool own = (lane & 12) == 0;
      unsigned long long miss = __ballot(own && (mag >= TB_N || !hot));
#pragma unroll 1
      for (; miss; miss &= miss - 1ull) {
        const int src = __builtin_ctzll(miss);
        const float t = rdl(tn, src);
        v2f ac = {0.f, 0.f};
#pragma unroll 4
        for (int i = 0; i < 12; ++i) { const LAS v2f* mw = (const LAS v2f*)(L + L_MLP) + i * 64 + lane; ac += gelu2(t * mw[0] + mw[768]) * mw[1536]; }
        const float yd = rbf(rbf(wave_sum(ac.x + ac.y)) + b2);
        yv = lane == src ? yd : yv; }
      if (own) ((__bf16*)a.out)[j * 640 + rcol] = (__bf16)(hv + yv);
      if (wg == 0 && lane == 0) __builtin_amdgcn_raw_buffer_store_b64(v2u{T.lo, T.hi}, rws, 0, 0, SC1); } }
}

extern "C" void kernel_launch(void* const* d_in, const int* in_sizes, int n_in, void* d_out, int out_size, void* d_ws, size_t ws_size, hipStream_t stream) {
  Args a{d_in[0], d_in[1], d_in[2], d_in[3], d_in[4], d_in[6], d_in[7], d_in[8], d_in[9], d_out, d_ws};
  fused_decode<<<256, 512, 0, stream>>>(a);
}
```

```cpp
#include <hip/hip_runtime.h>
#include <hip/hip_bf16.h>


#define LAS __attribute__((address_space(3)))
typedef unsigned v4u __attribute__((ext_vector_type(4)));
typedef unsigned v2u __attribute__((ext_vector_type(2)));
typedef float v4f __attribute__((ext_vector_type(4)));
typedef float v2f __attribute__((ext_vector_type(2)));
typedef short v4s __attribute__((ext_vector_type(4)));
typedef __bf16 v8b __attribute__((ext_vector_type(8)));
typedef __bf16 v2b __attribute__((ext_vector_type(2)));
typedef __amdgpu_buffer_rsrc_t rsrc_t;

namespace {
constexpr int LLO = 5, LMID = 7, LMAX = 9, NPRE = 8;
constexpr int BLO = 20, BHI = 40, TAUB = 248, DLY = 200, CONV = 124;
constexpr int NREP = 8, CSTR = 256, NRED = 40;
constexpr unsigned SLAB = 4096u * 384u, KVB = 128u * SLAB;
constexpr unsigned WS_QP = 4096, WS_OP = WS_QP + 256 * 1024, WS_YP = WS_OP + 4 * 256 * 1024, WS_TB = WS_YP + 128 * 2560, WS_SP = WS_TB + 256 * 64,
                   WS_CP = WS_SP + 4 * NREP * 640, WS_END = WS_CP + 3 * 32 * CSTR;
constexpr int L_Q = 0, L_FLAG = 1536, L_ML = 1664, L_OB = 1792, L_RED = L_OB + 4 * 384, L_PART = L_RED + 512, L_MLP = L_PART + 16 * 768, L_STG = L_MLP + 3 * 6144,
              L_TB = L_STG + 4 * 7680, L_END = L_TB + 16384;
constexpr int F_PART = 0, F_Q = 4, F_TB = 8, F_WO = 9, F_STEP = 10, F_HOT = 11, F_TAU = 12, F_BASE = 13, F_TIME = 16, F_ML = 24, F_QS = 28, F_TAG = 29;
constexpr unsigned TB_LO = 102u * 128u, TB_N = 28u * 128u;
constexpr int MASKED = 0x40000000, NT = 2, SC1 = 16;
constexpr int SPIN = 1 << 15;
constexpr float LOG2E = 1.4426950408889634f;

__device__ __forceinline__ rsrc_t mk(const void* p, unsigned n) { return __builtin_amdgcn_make_buffer_rsrc((void*)p, 0, (int)n, 0x00020000); }
__device__ __forceinline__ float asf(unsigned u) { return __builtin_bit_cast(float, u); }
__device__ __forceinline__ unsigned asu(float f) { return __builtin_bit_cast(unsigned, f); }
__device__ __forceinline__ float lo16(unsigned u) { return asf(u << 16); }
__device__ __forceinline__ float hi16(unsigned u) { return asf(u & 0xffff0000u); }
__device__ __forceinline__ unsigned pk(float a, float b) { return __builtin_bit_cast(unsigned, v2b{(__bf16)a, (__bf16)b}); }
__device__ __forceinline__ float rbf(float x) { return (float)(__bf16)x; }
__device__ __forceinline__ float dot2(unsigned a, unsigned b, float c) {
  return __builtin_amdgcn_fdot2_f32_bf16(__builtin_bit_cast(v2b, a), __builtin_bit_cast(v2b, b), c, false); }
template <int C> __device__ __forceinline__ float dpp(float x) { return asf(__builtin_amdgcn_mov_dpp(asu(x), C, 0xf, 0xf, true)); }
__device__ __forceinline__ float sw16(float a, float b) { auto r = __builtin_amdgcn_permlane16_swap(asu(a), asu(b), false, false); return asf(r[0]) + asf(r[1]); }
__device__ __forceinline__ float sw32(float a, float b) { auto r = __builtin_amdgcn_permlane32_swap(asu(a), asu(b), false, false); return asf(r[0]) + asf(r[1]); }
__device__ __forceinline__ float row_sum(float x) { x += dpp<0x128>(x); x += dpp<0x124>(x); x += dpp<0x122>(x); x += dpp<0x121>(x); return x; }
__device__ __forceinline__ float wave_sum(float x) { x = row_sum(x); x = sw16(x, x); return sw32(x, x); }
__device__ __forceinline__ float exp2_(float x) { return __builtin_amdgcn_exp2f(x); }
__device__ __forceinline__ float recip(float x) { const float r = __builtin_amdgcn_rcpf(x); return fmaf(fmaf(-x, r, 1.f), r, r); }
__device__ __forceinline__ float rdl(float x, int l) { return asf(__builtin_amdgcn_readlane(asu(x), l)); }
__device__ __forceinline__ unsigned clock10() { return (unsigned)__builtin_amdgcn_s_memrealtime(); }
__device__ __forceinline__ void ln_consts(float mean, float var, float& m, float& rinv) {
  m = rbf(mean); rinv = recip(rbf(__builtin_amdgcn_sqrtf(rbf(rbf(var) + 1e-5f)))); }
__device__ __forceinline__ v2f gelu2(v2f u) {
  const v2f z = u * (u * u * (-0.044715f * 1.5957691216057308f * LOG2E) + (-1.5957691216057308f * LOG2E));
  const v2f d = v2f{exp2_(z.x), exp2_(z.y)} + 1.f;
  return u * v2f{__builtin_amdgcn_rcpf(d.x), __builtin_amdgcn_rcpf(d.y)}; }

struct Tag { unsigned lo, hi; v4u t; };
__device__ __forceinline__ Tag make_tag(unsigned long long e) {
  const unsigned long long t = e + 1ull;
  return Tag{(unsigned)t, (unsigned)(t >> 32), v4u{(unsigned)t & 1023u, (unsigned)(t >> 10) & 1023u, (unsigned)(t >> 20) & 1023u, (unsigned)(t >> 30) & 1023u}}; }
__device__ __forceinline__ unsigned bfi(unsigned m, unsigned a, unsigned b) { unsigned r; asm("v_bfi_b32 %0, %1, %2, %3" : "=v"(r) : "v"(m), "s"(a), "v"(b)); return r; }
__device__ __forceinline__ v4u pack4(v4f v, const Tag& T) {
  return v4u{bfi(1023u, T.t.x, asu(v.x) + 512u), bfi(1023u, T.t.y, asu(v.y) + 512u), bfi(1023u, T.t.z, asu(v.z) + 512u), bfi(1023u, T.t.w, asu(v.w) + 512u)}; }
__device__ __forceinline__ unsigned or4(v4u x) { return (x.x | x.y) | (x.z | x.w); }

#define FENCE() asm volatile("" ::: "memory")
#define SB() __builtin_amdgcn_sched_barrier(0)
#define LDS_WAIT() asm volatile("s_waitcnt lgkmcnt(0)" ::: "memory")
#define BARRIER() asm volatile("s_waitcnt lgkmcnt(0)\n\ts_barrier" ::: "memory")
#define VM_WAIT(n) do { asm volatile("s_waitcnt vmcnt(%0)" ::"n"(n) : "memory"); SB(); } while (0)

template <int SLEEP = 0> __device__ __forceinline__ void lds_wait_ge(LAS unsigned char* L, int word, unsigned want) {
  volatile LAS unsigned* p = (volatile LAS unsigned*)(L + L_FLAG) + word;
#pragma unroll 1
  for (int i = 0; i < (SPIN << 5); ++i) { if (__builtin_amdgcn_readfirstlane(*p) >= want) break; if (SLEEP) __builtin_amdgcn_s_sleep(SLEEP); }
  FENCE(); }
__device__ __forceinline__ void lds_add(LAS unsigned char* L, int word, int lane) {
  LDS_WAIT();
  if (lane == 0) __hip_atomic_fetch_add((LAS unsigned*)(L + L_FLAG) + word, 1u, __ATOMIC_RELAXED, __HIP_MEMORY_SCOPE_WORKGROUP); }
}

struct Args { const void *x, *k, *v, *wq, *wo, *w1, *b1, *w2, *b2; void *out, *ws; };

__global__ void __launch_bounds__(512, 2) fused_decode(Args a) {
  __shared__ __attribute__((aligned(16))) unsigned char lds_raw[L_END];
  LAS unsigned char* L = (LAS unsigned char*)lds_raw;
  volatile LAS unsigned* fl = (volatile LAS unsigned*)(L + L_FLAG);
  const int wg = blockIdx.x, lane = threadIdx.x & 63, h = wg & 31, m = wg >> 5, hs = (h + 1) & 31;
  const int wave = __builtin_amdgcn_readfirstlane(threadIdx.x >> 6);
  asm volatile("" : "+s"(a.x), "+s"(a.k), "+s"(a.v), "+s"(a.wq), "+s"(a.ws));

  if (wave < 4) {
    const int w = wave;
    __builtin_amdgcn_s_setprio(2);
    const unsigned loff = (lane >> 3) * 384 + (lane & 7) * 16;
    const unsigned wbase = h * SLAB;
    unsigned go[4];
#pragma unroll
    for (int r = 0; r < 4; ++r) { const unsigned p = r * 32 + w * 8 + m, blk = (p * 1525u) >> 16, q = p - 43u * blk;
      go[r] = ((q >> 1) + (unsigned)((0x566B2B401600ull >> (16u * blk + 8u * (q & 1u))) & 255u)) * 3072u; }
    const rsrc_t rk = mk(a.k, KVB), rv = mk(a.v, KVB);
    v4u ring[4][3], pre[NPRE][3];
    auto ld = [&](const rsrc_t& rs, unsigned so, int n) { return __builtin_amdgcn_raw_buffer_load_b128(rs, loff + (n % 3) * 128, so + (n / 12) * 393216 + go[n / 3 % 4], NT); };
    auto issue = [&](int g, const rsrc_t& r0, unsigned s0, const rsrc_t& r1, unsigned s1) {
#pragma unroll
      for (int n = 3 * g + LMAX; n < 3 * g + LMAX + 3; ++n) ring[(n / 3) % 4][n % 3] = n < 48 ? ld(r0, s0, n) : ld(r1, s1, n - 48);
      SB(); };
    const unsigned woff = lane < 60 ? (lane / 10) * 1280 + (lane % 10) * 16 : MASKED;
    auto issue_v = [&](int g, bool last, unsigned s0, const rsrc_t& r1, unsigned s1) {
#pragma unroll
      for (int n = 3 * g + LMAX; n < 3 * g + LMAX + 3; ++n) {
        const int k = n - 48;
        ring[(n / 3) % 4][n % 3] = n < 48 ? ld(rv, s0, n)
            : __builtin_amdgcn_raw_buffer_load_b128(r1, last ? woff : loff + (k % 3) * 128, s1 + (last ? (k < 8 ? k * 7680u : (unsigned)MASKED) : go[k / 3]), NT); }
      SB(); };
    if (w == 0 && lane < 32) fl[lane] = 0u;
    BARRIER(); SB();
#pragma unroll
    for (int n = 0; n < 3 * NPRE + LMAX; ++n) {
      if (n >= LMID) VM_WAIT(LMID - 1);
      if (n < 3 * NPRE) pre[n / 3][n % 3] = ld(rk, wbase, n); else ring[(n / 3) % 4][n % 3] = ld(rk, wbase, n);
      SB(); }
    BARRIER();
    v4u q[3];
    float s[16];
    auto loadq = [&](int b) {
#pragma unroll
      for (int j = 0; j < 3; ++j) q[j] = *(const LAS v4u*)(L + L_Q + b * 384 + (lane & 7) * 16 + j * 128); };
    auto score = [&](const v4u(&r)[3]) -> float {
      float d0 = 0.f, d1 = 0.f, d2 = 0.f;
#pragma unroll
      for (int k = 0; k < 4; ++k) { d0 = dot2(r[0][k], q[0][k], d0); d1 = dot2(r[1][k], q[1][k], d1); d2 = dot2(r[2][k], q[2][k], d2); }
      float d = (d0 + d1) + d2;
      d += dpp<0xB1>(d); d += dpp<0x4E>(d); d += dpp<0x141>(d);
      d = rbf(rbf(d) * 0.07216878364870322f);
      asm volatile("" : "+v"(d));
      return d; };
    int mode = 1;
    auto wait_step = [&] { if (mode == 0) VM_WAIT(LLO - 3); else if (mode == 1) VM_WAIT(LMID - 3); else VM_WAIT(LMAX - 3); };
    unsigned now, tauv, basev;
    auto pace_a = [&](int g) { if ((g & 1) == 0) { now = clock10(); tauv = fl[F_TAU]; basev = fl[F_BASE]; SB(); } };
    auto pace_b = [&](int p, int g) { if ((g & 1) == 0) {
      const int n = p * 16 + g;
      if (w == 0 && lane == 0) { if (g == 0) fl[F_TIME + p] = now; fl[F_STEP] = n; }
      const int tau = __builtin_amdgcn_readfirstlane(tauv), lead = (int)((unsigned)__builtin_amdgcn_readfirstlane(basev) + (unsigned)((n * tau) >> 8) - now);
      mode = tau == 0 ? 1 : lead > BLO ? 0 : lead < -BHI ? 2 : 1; } };
    loadq(0);
#pragma unroll
    for (int g = NPRE; g < 16; ++g) {
      VM_WAIT(LMID - 3);
      issue(g, rk, wbase, rv, wbase);
      s[g] = score(ring[g % 4]);
      s[g - NPRE] = score(pre[g - NPRE]);
      SB(); }
    const int b3 = (lane >> 3) & 1, a0 = 12 * (lane >> 5) + 6 * ((lane >> 4) & 1) + 3 * b3;
#pragma unroll 1
    for (int b = 0;; ++b) {
      const unsigned sb = wbase + b * (32 * SLAB);
      const rsrc_t rn = mk(b < 3 ? a.k : a.wo, b < 3 ? KVB : 6144u * 1280u);
      const unsigned nb = b < 3 ? sb + 32 * SLAB : (hs * 192 + w * 48) * 1280 + m * 160;
      v2f ac2[12];
#pragma unroll
      for (int g = 0; g < 16; ++g) {
        wait_step();
        issue_v(g, b == 3, sb, rn, nb);
        pace_a(g);
        if (g == 0) {
          float mx = s[0], l = 0.f;
#pragma unroll
          for (int i = 1; i < 16; ++i) mx = fmaxf(mx, s[i]);
          mx = fmaxf(mx, dpp<0x128>(mx));
          { auto r = __builtin_amdgcn_permlane16_swap(asu(mx), asu(mx), false, false); mx = fmaxf(asf(r[0]), asf(r[1])); }
          { auto r = __builtin_amdgcn_permlane32_swap(asu(mx), asu(mx), false, false); mx = fmaxf(asf(r[0]), asf(r[1])); }
#pragma unroll
          for (int i = 0; i < 16; ++i) { s[i] = exp2_((s[i] - mx) * LOG2E); l += s[i]; }
          l += dpp<0x128>(l); l = sw16(l, l); l = sw32(l, l);
          if (lane == 0) *(LAS v2f*)(L + L_ML + (b * 4 + w) * 8) = v2f{mx, l};
          lds_add(L, F_ML + b, lane);
#pragma unroll
          for (int k = 0; k < 12; ++k) ac2[k] = v2f{0.f, 0.f}; }
#pragma unroll
        for (int k = 0; k < 12; ++k) { const unsigned u = ring[g % 4][k / 4][k % 4];
          ac2[k] += s[g] * v2f{lo16(u), hi16(u)};
          asm volatile("" : "+v"(ac2[k])); }
        pace_b(2 * b + 1, g);
        if (g >= 12 && b == 3) mode = 2;
        SB(); }
      float acc[12];
#pragma unroll
      for (int i = 0; i < 12; ++i) acc[i] = sw32(ac2[i / 2][i % 2], ac2[6 + i / 2][i % 2]);
#pragma unroll
      for (int i = 0; i < 6; ++i) acc[i] = sw16(acc[i], acc[i + 6]);
      LAS float* part = (LAS float*)(L + L_PART + (b * 4 + w) * 768);
#pragma unroll
      for (int t = 0; t < 3; ++t) { const float u0 = acc[t] + dpp<0x128>(acc[t]), u1 = acc[t + 3] + dpp<0x128>(acc[t + 3]);
        const int ai = a0 + t; part[64 * (ai >> 3) + 8 * (lane & 7) + (ai & 7)] = b3 ? u1 : u0; }
      lds_add(L, F_PART + b, lane);
      if (b == 3) break;
      lds_wait_ge(L, F_Q + b + 1, 1u);
      loadq(b + 1);
#pragma unroll
      for (int g = 0; g < 16; ++g) {
        wait_step();
        issue(g, rk, nb, rv, nb);
        pace_a(g);
        s[g] = score(ring[g % 4]);
        pace_b(2 * b + 2, g);
        SB(); }
    }
    if (lane < 60) {
#pragma unroll
      for (int k = 0; k < 8; ++k) *(LAS v4u*)(L + L_STG + (w * 8 + k) * 960 + lane * 16) = ring[k / 3][k % 3]; }
    lds_add(L, F_WO, lane);
    { const rsrc_t rw = mk(a.ws, WS_END);
      unsigned z = __builtin_amdgcn_raw_buffer_load_b32(rw, lane < 5 ? lane * 64 : MASKED, WS_YP + (w * 32 + hs) * 2560 + m * 320, SC1);
      if (w == 1) z |= __builtin_amdgcn_raw_buffer_load_b32(rw, (wg & 63) < NRED && lane < 10 ? lane * 64 : MASKED, WS_SP + ((wg >> 6) * NREP + (wg & (NREP - 1))) * 640, SC1);
      asm volatile("" :: "v"(z));
      if ((wg & 63) < NRED) {
        const unsigned tag = fl[F_TAG];
        v4u t[4]; unsigned bad = 0;
#pragma unroll
        for (int p = 0; p < 4; ++p) t[p] = __builtin_amdgcn_raw_buffer_load_b128(rw, lane * 16, WS_TB + (w * 4 + p) * 1024, SC1);
#pragma unroll
        for (int p = 0; p < 4; ++p) { bad |= (t[p].w >> 16) ^ tag; *(LAS v4u*)(L + L_TB + (w * 4 + p) * 1024 + lane * 16) = t[p]; }
        LDS_WAIT();
        const unsigned add = __all(bad == 0u) ? 0x10001u : 0x10000u;
        if (lane == 0) __hip_atomic_fetch_add((LAS unsigned*)(L + L_FLAG) + F_HOT, add, __ATOMIC_RELAXED, __HIP_MEMORY_SCOPE_WORKGROUP); } }
  } else {
    const int j = wave - 4, c = lane & 15, qd = lane >> 4;
    const rsrc_t rws = mk(a.ws, WS_END), rx = mk(a.x, 5120);
    LAS unsigned char* stg = L + L_STG + j * 7680;
    unsigned long long e0 = *(const __attribute__((address_space(4))) unsigned long long*)a.ws;
    v4u xr[5], wr[8]; v2u xo[5];
#pragma unroll
    for (int i = 0; i < 5; ++i) xr[i] = __builtin_amdgcn_raw_buffer_load_b128(rx, qd * 1280 + c * 16 + i * 256, 0, 0);
#pragma unroll
    for (int i = 0; i < 5; ++i) xo[i] = __builtin_amdgcn_raw_buffer_load_b64(rx, (c >> 2) * 1280 + qd * 8 + i * 32, ((m >> 2) * 4 + j) * 160, 0);
    SB();
    { const rsrc_t rq = mk(a.wq, 640 * 12288);
      const int off = lane < 60 ? (lane / 6) * 12288 + (lane % 6) * 16 : MASKED;
#pragma unroll
      for (int i = 0; i < 8; ++i) wr[i] = __builtin_amdgcn_raw_buffer_load_b128(rq, off, (((m >> 2) * 4 + j) * 80 + i * 10) * 12288 + hs * 384 + (m & 3) * 96, NT); }
    SB();
    __builtin_amdgcn_s_barrier();
    SB(); FENCE();
    float sum = 0.f;
#pragma unroll
    for (int i = 0; i < 5; ++i)
#pragma unroll
      for (int k = 0; k < 4; ++k) sum += lo16(xr[i][k]) + hi16(xr[i][k]);
    const float mean0 = row_sum(sum) * (1.f / 640.f);
    float sq = 0.f;
#pragma unroll
    for (int i = 0; i < 5; ++i)
#pragma unroll
      for (int k = 0; k < 4; ++k) { const float d0 = lo16(xr[i][k]) - mean0, d1 = hi16(xr[i][k]) - mean0; sq = fmaf(d0, d0, fmaf(d1, d1, sq)); }
    float mm, rinv; ln_consts(mean0, row_sum(sq) * (1.f / 640.f), mm, rinv);
    { const int src = (c >> 2) * 64; mm = asf(__builtin_amdgcn_ds_bpermute(src, asu(mm))); rinv = asf(__builtin_amdgcn_ds_bpermute(src, asu(rinv))); }
    v4s xb[5];
#pragma unroll
    for (int i = 0; i < 5; ++i) {
      xb[i] = __builtin_bit_cast(v4s, v2u{pk(rbf(lo16(xo[i].x) - mm) * rinv, rbf(hi16(xo[i].x) - mm) * rinv), pk(rbf(lo16(xo[i].y) - mm) * rinv, rbf(hi16(xo[i].y) - mm) * rinv)});
      asm volatile("" : "+v"(xb[i])); }
    asm volatile("" : "+s"(e0));
    SB();
    const Tag T = make_tag(e0);
    if (lane < 60) {
#pragma unroll
      for (int i = 0; i < 8; ++i) *(LAS v4u*)(stg + i * 960 + lane * 16) = wr[i]; }
    { const int troff = (4 * qd + (c >> 2)) * 96 + (c & 3) * 8;
      v4f d[3];
#pragma unroll
      for (int t = 0; t < 3; ++t) {
        d[t] = v4f{0.f, 0.f, 0.f, 0.f};
#pragma unroll
        for (int i = 0; i < 5; ++i)
          d[t] = __builtin_amdgcn_mfma_f32_16x16x16bf16_1k(xb[i], __builtin_amdgcn_ds_read_tr16_b64_v4i16((LAS v4s*)(stg + troff + i * 1536 + t * 32)), d[t], 0, 0, 0); }
      LAS v4f* qp = (LAS v4f*)(L + L_PART);
      qp[j * 64 + lane] = v4f{d[0].x, d[1].x, d[2].x, 0.f};
      lds_add(L, F_QS, lane);
      if (j == 0) {
        lds_wait_ge(L, F_QS, 4u);
        if (lane == 0) fl[F_TAG] = T.lo & 0xffffu;
        const v4f t = (qp[lane] + qp[64 + lane]) + (qp[128 + lane] + qp[192 + lane]);
        __builtin_amdgcn_raw_buffer_store_b128(v4u{asu(t.x), asu(t.y), asu(t.z), T.lo}, rws, lane * 16, WS_QP + (hs * 8 + m) * 1024, SC1); } }
    if (j > 0) { BARRIER(); __builtin_amdgcn_s_sleep(8); } else __builtin_amdgcn_s_sleep(40);
    { v4u g0, g1;
#pragma unroll 1
      for (int i = 0; i < SPIN; ++i) {
        FENCE();
        g0 = __builtin_amdgcn_raw_buffer_load_b128(rws, qd * 1024 + c * 16, WS_QP + h * 8192 + j * 256, SC1);
        g1 = __builtin_amdgcn_raw_buffer_load_b128(rws, qd * 1024 + c * 16, WS_QP + h * 8192 + 4096 + j * 256, SC1);
        if (__all(g0.w == T.lo && g1.w == T.lo)) break;
        __builtin_amdgcn_s_sleep(8); }
      LAS __bf16* qs = (LAS __bf16*)(L + L_Q + j * 384) + 48 * qd + c;
      qs[0] = (__bf16)(asf(g0.x) + asf(g1.x)); qs[16] = (__bf16)(asf(g0.y) + asf(g1.y)); qs[32] = (__bf16)(asf(g0.z) + asf(g1.z)); }
    if (j == 0) BARRIER(); else lds_add(L, F_Q + j, lane);
    const float b2 = lo16(*(const unsigned short*)a.b2);
    if (j < 3) {
      { const rsrc_t r1 = mk(a.w1, 3072), r2 = mk(a.b1, 3072), r3 = mk(a.w2, 3072);
        unsigned mr[12];
#pragma unroll
        for (int i = 0; i < 4; ++i) { mr[i] = __builtin_amdgcn_raw_buffer_load_b32(r1, lane * 4 + i * 256, j * 1024, 0);
          mr[4 + i] = __builtin_amdgcn_raw_buffer_load_b32(r2, lane * 4 + i * 256, j * 1024, 0);
          mr[8 + i] = __builtin_amdgcn_raw_buffer_load_b32(r3, lane * 4 + i * 256, j * 1024, 0); }
#pragma unroll
        for (int i = 0; i < 12; ++i) *(LAS v2f*)(L + L_MLP + (i / 4) * 6144 + j * 2048 + (i % 4) * 512 + lane * 8) = v2f{lo16(mr[i]), hi16(mr[i])}; }
      const unsigned e = wg * 28 + min(c, 13) * 2, neg = e >= TB_N, bits = (e - neg * TB_N + TB_LO) | (neg << 15);
      const v2f t2 = {lo16(bits), lo16(bits + 1u)};
      v2f ac = {0.f, 0.f};
      const LAS v4f* mw = (const LAS v4f*)(L + L_MLP + j * 2048 + qd * 512);
#pragma unroll 2
      for (int i = 0; i < 32; ++i) {
        const v4f w1 = mw[i], b1 = mw[384 + i], w2 = mw[768 + i];
#pragma unroll
        for (int k = 0; k < 4; ++k) ac += gelu2(t2 * w1[k] + b1[k]) * w2[k]; }
      ac.x = sw16(ac.x, ac.x); ac.y = sw16(ac.y, ac.y); ac.x = sw32(ac.x, ac.x); ac.y = sw32(ac.y, ac.y);
      LAS v2f* red = (LAS v2f*)(L + L_RED);
      if (lane < 16) red[j * 16 + lane] = ac;
      lds_add(L, F_TB, lane);
      if (j == 0) {
        lds_wait_ge<1>(L, F_TB, 3u);
        const v2f y = (red[c] + red[16 + c]) + red[32 + c];
        LAS unsigned short* tg = (LAS unsigned short*)(L + L_RED + 384);
        const unsigned yb = pk(rbf(y.x) + b2, rbf(y.y) + b2);
        if (lane < 14) { tg[(2 * c) / 7 * 8 + (2 * c) % 7] = (unsigned short)yb; tg[(2 * c + 1) / 7 * 8 + (2 * c + 1) % 7] = (unsigned short)(yb >> 16); }
        if (lane < 4) tg[lane * 8 + 7] = (unsigned short)T.lo;
        __builtin_amdgcn_raw_buffer_store_b128(*(const LAS v4u*)(tg + (lane & 3) * 8), rws, lane < 4 ? lane * 16 : MASKED, WS_TB + wg * 64, SC1); }
    } else {
      unsigned prev = 0;
#pragma unroll 1
      for (int k = 0; k < 3; ++k) {
        const int n0 = 32 * (k + 1);
        lds_wait_ge<2>(L, F_STEP, n0);
        const unsigned own = __builtin_amdgcn_readfirstlane(fl[F_TIME + 2 * k + 2]);
        __builtin_amdgcn_raw_buffer_store_b64(v2u{own, T.lo}, rws, lane == 0 ? (wg >> 3) * CSTR + (wg & 7) * 8 : MASKED, WS_CP + k * 32 * CSTR, SC1);
        float sum = 0.f; int cnt = 0;
#pragma unroll 1
        for (int tries = 0; tries < 2 && cnt < 32; ++tries) {
#pragma unroll 1
          for (int it = 0; it < SPIN; ++it) { if ((int)(clock10() - own) >= DLY + 60 * tries) break; __builtin_amdgcn_s_sleep(4); }
          FENCE();
          const v2u e = __builtin_amdgcn_raw_buffer_load_b64(rws, lane < 32 ? (((wg >> 3) + (lane & 24)) & 31) * CSTR + (lane & 7) * 8 : MASKED, WS_CP + k * 32 * CSTR, SC1);
          const bool ok = lane < 32 && e.y == T.lo;
          cnt = __builtin_popcountll(__ballot(ok));
          sum = wave_sum(ok ? (float)(int)(e.x - own) : 0.f); }
        const float lag = cnt ? sum * __builtin_amdgcn_rcpf((float)cnt) : 0.f;
        const unsigned mean = own + (unsigned)__builtin_amdgcn_readfirstlane((int)lag);
        if (k > 0) { const int tau = (((int)(mean - prev) * TAUB) >> 5) + (int)(lag * 256.f * __builtin_amdgcn_rcpf((float)(CONV - n0)));
          if (lane == 0) { fl[F_BASE] = own - (unsigned)((n0 * tau) >> 8); fl[F_TAU] = tau; } }
        prev = mean; } }
    const int rj = wg - 64 * j, rm = (rj & 63) / 5, rs5 = (rj & 63) - 5 * rm, rcol = 80 * rm + (rs5 < 4 ? 16 * qd + 4 * rs5 + (lane & 3) : 64 + 4 * (lane & 3) + qd);
    const bool reducer = rj >= 0 && rj < NRED;
    const float xv = lo16(__builtin_amdgcn_raw_buffer_load_b16(rx, reducer ? rcol * 2 : MASKED, j * 1280, 0));
    float sc[4], M, ls; v4u sgr;
    { lds_wait_ge<4>(L, F_ML + j, 4u);
      const v4f p0 = *(const LAS v4f*)(L + L_ML + j * 32), p1 = *(const LAS v4f*)(L + L_ML + j * 32 + 16);
      M = fmaxf(fmaxf(p0.x, p0.z), fmaxf(p1.x, p1.z));
      sc[0] = exp2_((p0.x - M) * LOG2E); sc[1] = exp2_((p0.z - M) * LOG2E); sc[2] = exp2_((p1.x - M) * LOG2E); sc[3] = exp2_((p1.z - M) * LOG2E);
      ls = fmaf(sc[3], p1.w, fmaf(sc[2], p1.y, fmaf(sc[1], p0.w, sc[0] * p0.y)));
      sgr = pack4(v4f{M, ls, 0.f, 0.f}, T);
      asm volatile("" : "+v"(sc[0]), "+v"(sc[1]), "+v"(sc[2]), "+v"(sc[3]), "+v"(sgr)); }
    __builtin_amdgcn_s_setprio(3);
    lds_wait_ge(L, F_PART + 3, 4u);
    { const LAS v2f* pp = (const LAS v2f*)(L + L_PART + j * 3072) + (lane & 31) * 3;
      unsigned u[3];
#pragma unroll
      for (int i = 0; i < 3; ++i) { const v2f o = ((sc[0] * pp[i] + sc[1] * pp[96 + i]) + sc[2] * pp[192 + i]) + sc[3] * pp[288 + i]; u[i] = pk(o.x, o.y); }
      __builtin_amdgcn_raw_buffer_store_b128(lane < 32 ? v4u{u[0], u[1], u[2], T.lo} : sgr, rws, lane < 33 ? lane * 16 : MASKED, WS_OP + ((j * 32 + h) * 8 + m) * 1024, SC1); }
    v8b wf[6][5];
    { lds_wait_ge(L, F_WO, 4u);
      const int tro = L_STG + (8 * qd + (c >> 2)) * 160 + (c & 3) * 8;
#pragma unroll
      for (int s = 0; s < 6; ++s)
#pragma unroll
        for (int t = 0; t < 5; ++t) {
          const v4s p0 = __builtin_amdgcn_ds_read_tr16_b64_v4i16((LAS v4s*)(L + tro + s * 5120 + t * 32));
          const v4s p1 = __builtin_amdgcn_ds_read_tr16_b64_v4i16((LAS v4s*)(L + tro + s * 5120 + t * 32 + 640));
          wf[s][t] = __builtin_bit_cast(v8b, __builtin_shufflevector(p0, p1, 0, 1, 2, 3, 4, 5, 6, 7)); } }
    { v4u g[4]; float wk;
      const int hf = lane >> 5, p8 = lane & 7, poff = hf * 1024 + (lane & 31) * 16, soff = (2 * (p8 & 3) + ((p8 >> 2) ^ hf)) * 1024 + 512;
#pragma unroll 1
      for (int i = 0; i < SPIN; ++i) {
        FENCE();
        const v4u gs = __builtin_amdgcn_raw_buffer_load_b128(rws, soff, WS_OP + (j * 32 + hs) * 8192, SC1) ^ T.t;
#pragma unroll
        for (int k = 0; k < 4; ++k) g[k] = __builtin_amdgcn_raw_buffer_load_b128(rws, poff, WS_OP + (j * 32 + hs) * 8192 + k * 2048, SC1);
        SB();
        const float Mk = asf(gs.x);
        float Mg = fmaxf(Mk, dpp<0xB1>(Mk)); Mg = fmaxf(Mg, dpp<0x4E>(Mg)); Mg = fmaxf(Mg, dpp<0x141>(Mg));
        wk = exp2_((Mk - Mg) * LOG2E);
        float Lg = wk * asf(gs.y);
        Lg += dpp<0xB1>(Lg); Lg += dpp<0x4E>(Lg); Lg += dpp<0x141>(Lg);
        wk *= recip(Lg);
        asm volatile("" : "+v"(wk));
        SB();
        if (__all((((g[0].w ^ T.lo) | (g[1].w ^ T.lo)) | ((g[2].w ^ T.lo) | (g[3].w ^ T.lo)) | (or4(gs) & 1023u)) == 0u)) break; }
      const float w4[4] = {dpp<0x150>(wk), dpp<0x151>(wk), dpp<0x152>(wk), dpp<0x153>(wk)};
      v2f o[3] = {{0.f, 0.f}, {0.f, 0.f}, {0.f, 0.f}};
#pragma unroll
      for (int k = 0; k < 4; ++k)
#pragma unroll
        for (int i = 0; i < 3; ++i) o[i] += w4[k] * v2f{lo16(g[k][i]), hi16(g[k][i])};
      const float oa = sw32(o[0].x, o[1].x), ob2 = sw32(o[0].y, o[1].y), oc = sw32(o[2].x, o[2].y);
      LAS unsigned char* ob = L + L_OB + j * 384;
      *(LAS unsigned*)(ob + (lane & 31) * 12 + hf * 4) = pk(oa, ob2);
      *(LAS __bf16*)(ob + (lane & 31) * 12 + 8 + hf * 2) = (__bf16)oc;
      v4f y[5];
#pragma unroll
      for (int t = 0; t < 5; ++t) y[t] = v4f{0.f, 0.f, 0.f, 0.f};
#pragma unroll
      for (int s = 0; s < 6; ++s) {
        const v8b of = *(const LAS v8b*)(ob + s * 64 + qd * 16);
#pragma unroll
        for (int t = 0; t < 5; ++t) y[t] = __builtin_amdgcn_mfma_f32_16x16x32_bf16(of, wf[s][t], y[t], 0, 0, 0); }
      const v4f y4 = {dpp<0x00>(y[4].x), dpp<0x55>(y[4].x), dpp<0xAA>(y[4].x), dpp<0xFF>(y[4].x)};
      const v4f ys = qd == 0 ? v4f{y[0].x, y[1].x, y[2].x, y[3].x} : y4;
      __builtin_amdgcn_raw_buffer_store_b128(pack4(ys, T), rws, qd == 0 ? c * 16 : (qd == 1 && (c & 3) == 0) ? 256 + c * 4 : MASKED, WS_YP + (j * 32 + hs) * 2560 + m * 320, SC1); }
    if (reducer) {
      const int yoff = (lane >> 2) * 2560 + (lane & 3) * 16;
      v4u g0, g1;
#pragma unroll 1
      for (int i = 0; i < SPIN; ++i) {
        FENCE();
        g0 = __builtin_amdgcn_raw_buffer_load_b128(rws, yoff, WS_YP + j * 32 * 2560 + rj * 64, SC1) ^ T.t;
        g1 = __builtin_amdgcn_raw_buffer_load_b128(rws, yoff, WS_YP + (j * 32 + 16) * 2560 + rj * 64, SC1) ^ T.t;
        if (__all(((or4(g0) | or4(g1)) & 1023u) == 0u)) break; }
      const v4f y4 = __builtin_bit_cast(v4f, g0) + __builtin_bit_cast(v4f, g1);
      float y = sw16(sw32(y4.x, y4.z), sw32(y4.y, y4.w));
      y += dpp<0x124>(y); y += dpp<0x128>(y);
      const float hv = rbf(xv + rbf(y));
      float s1 = hv, s2 = hv * hv;
      s1 += dpp<0xB1>(s1); s2 += dpp<0xB1>(s2); s1 += dpp<0x4E>(s1); s2 += dpp<0x4E>(s2);
      s1 = sw16(s1, s1); s2 = sw16(s2, s2); s1 = sw32(s1, s1); s2 = sw32(s2, s2);
      __builtin_amdgcn_raw_buffer_store_b128(v4u{asu(s1), asu(s2), T.lo, T.hi}, rws, lane < NREP ? lane * 640 : MASKED, WS_SP + j * NREP * 640 + rj * 16, SC1);
      lds_wait_ge(L, F_HOT, 4u << 16);
      const bool hot = __builtin_amdgcn_readfirstlane(fl[F_HOT]) == 0x40004u;
      v4u sg;
#pragma unroll 1
      for (int i = 0; i < SPIN; ++i) {
        FENCE();
        sg = __builtin_amdgcn_raw_buffer_load_b128(rws, lane < NRED ? lane * 16 : MASKED, WS_SP + (j * NREP + (wg & (NREP - 1))) * 640, SC1);
        if (__all((sg.z == T.lo && sg.w == T.hi) || lane >= NRED)) break; }
      const float mean = wave_sum(asf(sg.x)) * (1.f / 640.f), var = fmaxf(fmaf(wave_sum(asf(sg.y)), 1.f / 640.f, -mean * mean), 0.f);
      float m1, r1, m2, r2;
      ln_consts(mean, var, m1, r1);
      ln_consts((mean - m1) * r1, var * r1 * r1, m2, r2);
      const float tn = rbf(rbf(rbf(rbf(hv - m1) * r1) - m2) * r2);
      const unsigned tb = asu(tn) >> 16, mag = (tb & 0x7fffu) - TB_LO, e = min(mag, TB_N - 1u) + (tb >> 15) * TB_N, e7 = (e * 9363u) >> 16;
      float yv = lo16(*(const LAS unsigned short*)(L + L_TB + (e + e7) * 2));
      const bool own = (lane & 12) == 0;
      unsigned long long miss = __ballot(own && (mag >= TB_N || !hot));
      if (miss) lds_wait_ge(L, F_TB, 3u);
#pragma unroll 1
      for (; miss; miss &= miss - 1ull) {
        const int src = __builtin_ctzll(miss);
        const float t = rdl(tn, src);
        v2f ac = {0.f, 0.f};
#pragma unroll 4
        for (int i = 0; i < 12; ++i) { const LAS v2f* mw = (const LAS v2f*)(L + L_MLP) + i * 64 + lane; ac += gelu2(t * mw[0] + mw[768]) * mw[1536]; }
        const float yd = rbf(rbf(wave_sum(ac.x + ac.y)) + b2);
        yv = lane == src ? yd : yv; }
      if (own) ((__bf16*)a.out)[j * 640 + rcol] = (__bf16)(hv + yv);
      if (wg == 0 && lane == 0) __builtin_amdgcn_raw_buffer_store_b64(v2u{T.lo, T.hi}, rws, 0, 0, SC1); } }
}

extern "C" void kernel_launch(void* const* d_in, const int* in_sizes, int n_in, void* d_out, int out_size, void* d_ws, size_t ws_size, hipStream_t stream) {
  Args a{d_in[0], d_in[1], d_in[2], d_in[3], d_in[4], d_in[6], d_in[7], d_in[8], d_in[9], d_out, d_ws};
  fused_decode<<<256, 512, 0, stream>>>(a);
}
```
